```python
import torch

def _softmax(x, axis=-1):
    return torch.softmax(x, dim=axis)
def _gelu(x):
    return torch.nn.functional.gelu(x, approximate="tanh")
def _silu(x):
    return torch.nn.functional.silu(x)
def _sigmoid(x):
    return torch.sigmoid(x)
def _layernorm(x, axis=-1, eps=1e-5):
    m = x.mean(dim=axis, keepdim=True)
    v = x.var(dim=axis, keepdim=True, unbiased=False)
    return (x - m) / torch.sqrt(v + eps)
def _scatter_add(tgt, idx, src, axis=0):
    return tgt.clone().index_add_(axis, idx, src)
def _segment_sum(src, seg, n):
    out = torch.zeros((n,) + src.shape[1:], dtype=src.dtype)
    return out.index_add_(0, seg, src)
def _pad(x, pw):
    flat = tuple(v for pair in reversed(pw) for v in pair)
    return torch.nn.functional.pad(x, flat)

kernel_name = 'standalone__gqa_block_cached__near_ridge'

def setup_inputs():

    _w = (lambda a: (lambda t: t.to(torch.bfloat16) if t.is_floating_point() else t)(torch.as_tensor(a)))
    B, Sq, D = 8, 160, 448
    Hq, Hkv, Sk, Dh = 16, 8, 1024, 256
    assert Hq % Hkv == 0, (Hq, Hkv)
    return {
        "x": _w((lambda *s: torch.randn(*s, dtype=torch.bfloat16) * 0.8)(B, Sq, D)),
        "k_cache": _w((lambda *s: torch.randn(*s, dtype=torch.bfloat16) * 0.8)(B, Hkv, Sk, Dh)),
        "v_cache": _w((lambda *s: torch.randn(*s, dtype=torch.bfloat16) * 0.05)(B, Hkv, Sk, Dh)),
        "wq": _w((lambda *s: torch.randn(*s, dtype=torch.bfloat16) * 0.8)(D, Hq * Dh) * (D ** -0.5)),
        "wo": _w((lambda *s: torch.randn(*s, dtype=torch.bfloat16) * 0.05)(Hq * Dh, D) * ((Hq * Dh) ** -0.5)),
    }

def reference(x, k_cache, v_cache, wq, wo):
    B, Sq, D = x.shape
    Hq, Dh = 16, 256
    Hkv = k_cache.shape[1]
    rep = Hq // Hkv
    q = (lambda x, *d: x.permute(*d))((x @ wq).reshape(B, Sq, Hq, Dh), 0, 2, 1, 3)
    k = (lambda x, n, axis: torch.repeat_interleave(x, n, dim=axis))(k_cache, rep, axis=1)
    v = (lambda x, n, axis: torch.repeat_interleave(x, n, dim=axis))(v_cache, rep, axis=1)
    scores = torch.einsum("bhqd,bhkd->bhqk", q, k) * (Dh ** -0.5)
    p = _softmax(scores, axis=-1)
    o = torch.einsum("bhqk,bhkd->bhqd", p, v)
    return (lambda x, *d: x.permute(*d))(o, 0, 2, 1, 3).reshape(B, Sq, Hq * Dh) @ wo
```

```cpp
#include <hip/hip_runtime.h>
#include <hip/hip_bf16.h>
typedef __hip_bfloat16 bf16;
constexpr int B=8,SQ=160,D=448,HQ=16,HKV=8,SK=1024,DH=256;
__device__ inline float b2f(bf16 x){return __bfloat162float(x);}
__global__ void qproj(const bf16* x,const bf16* wq,float* q){
  int row=blockIdx.x;
  __shared__ float xs[D];
  for(int i=threadIdx.x;i<D;i+=256) xs[i]=b2f(x[row*D+i]);
  __syncthreads();
  for(int n=threadIdx.x;n<HQ*DH;n+=256){
    float a=0; for(int k=0;k<D;++k) a+=xs[k]*b2f(wq[(size_t)k*HQ*DH+n]);
    q[(size_t)row*HQ*DH+n]=b2f(__float2bfloat16(a));
  }
}
__global__ void attn(const float* q,const bf16* kc,const bf16* vc,float* o){
  int row=blockIdx.x, h=blockIdx.y; int b=row/SQ; int kvh=h/2;
  __shared__ float qs[DH]; __shared__ float p[SK]; __shared__ float red[256];
  int t=threadIdx.x;
  qs[t]=q[(size_t)row*HQ*DH+h*DH+t]; __syncthreads();
  const bf16* K=kc+((size_t)b*HKV+kvh)*SK*DH; const bf16* V=vc+((size_t)b*HKV+kvh)*SK*DH;
  float mx=-1e30f;
  for(int k=t;k<SK;k+=256){ float a=0; for(int d=0;d<DH;++d) a+=qs[d]*b2f(K[(size_t)k*DH+d]); a*=0.0625f; p[k]=a; mx=fmaxf(mx,a);}
  red[t]=mx; __syncthreads();
  for(int s=128;s>0;s>>=1){ if(t<s) red[t]=fmaxf(red[t],red[t+s]); __syncthreads(); }
  mx=red[0]; __syncthreads();
  float sm=0; for(int k=t;k<SK;k+=256){ float e=__expf(p[k]-mx); p[k]=e; sm+=e; }
  red[t]=sm; __syncthreads();
  for(int s=128;s>0;s>>=1){ if(t<s) red[t]+=red[t+s]; __syncthreads(); }
  sm=red[0];
  float a=0; for(int k=0;k<SK;++k) a+=p[k]*b2f(V[(size_t)k*DH+t]);
  o[(size_t)row*HQ*DH+h*DH+t]=b2f(__float2bfloat16(a/sm));
}
__global__ void oproj(const float* o,const bf16* wo,bf16* out){
  int row=blockIdx.x; __shared__ float os[HQ*DH];
  for(int i=threadIdx.x;i<HQ*DH;i+=448) os[i]=o[(size_t)row*HQ*DH+i];
  __syncthreads();
  int n=threadIdx.x; float a=0; for(int k=0;k<HQ*DH;++k) a+=os[k]*b2f(wo[(size_t)k*D+n]);
  out[row*D+n]=__float2bfloat16(a);
}
extern "C" void kernel_launch(void* const* d_in, const int* in_sizes, int n_in, void* d_out, int out_size, void* d_ws, size_t ws_size, hipStream_t stream){
  const bf16* x=(const bf16*)d_in[0]; const bf16* kc=(const bf16*)d_in[1]; const bf16* vc=(const bf16*)d_in[2];
  const bf16* wq=(const bf16*)d_in[3]; const bf16* wo=(const bf16*)d_in[4];
  float* q=(float*)d_ws; float* o=q+(size_t)B*SQ*HQ*DH;
  qproj<<<B*SQ,256,0,stream>>>(x,wq,q);
  attn<<<dim3(B*SQ,HQ),256,0,stream>>>(q,kc,vc,o);
  oproj<<<B*SQ,448,0,stream>>>(o,wo,(bf16*)d_out);
}
```

```cpp
#include <hip/hip_runtime.h>
#include <hip/hip_bf16.h>
#include <type_traits>
#pragma clang diagnostic ignored "-Wint-to-pointer-cast"

typedef __bf16 bf16x8 __attribute__((ext_vector_type(8)));
typedef __bf16 bf16x4 __attribute__((ext_vector_type(4)));
typedef __bf16 bf16x2 __attribute__((ext_vector_type(2)));
typedef float f32x4 __attribute__((ext_vector_type(4)));
typedef float f32x2 __attribute__((ext_vector_type(2)));
typedef unsigned u32x4 __attribute__((ext_vector_type(4)));
typedef unsigned u32x2 __attribute__((ext_vector_type(2)));
#define LAS __attribute__((address_space(3)))
#define DI __device__ __forceinline__
typedef __amdgpu_buffer_rsrc_t rsrc_t;

constexpr int NB = 8, SQ = 160, DM = 448, HQ = 16, HKV = 8, SK = 1024, DH = 256;
constexpr unsigned XROW = DM * 2, WQROW = HQ * DH * 2, KVROW = DH * 2, WOROW = DM * 2;
constexpr unsigned SLAB = 80 * DM * 2;
constexpr unsigned KSLOT = 8448;
constexpr unsigned L_K0 = 0, L_K1 = 4 * KSLOT, L_VR = 8 * KSLOT, L_P0 = L_VR + 65536, L_P1 = L_P0 + 10240, L_MS = L_P1 + 10240, L_TOTAL = L_MS + 10240;
static_assert(L_TOTAL == 163840, "LDS");
constexpr unsigned L_FLAG = L_MS;
constexpr unsigned L_DLT = L_MS + 64;
constexpr unsigned L_LS = L_VR + 3 * 16384 + 12288;
constexpr float QSCALE = 0.0625f * 1.4426950408889634f;
#define PTHR 65536.f

#define SBAR() __builtin_amdgcn_sched_barrier(0)
#define WAITVM(n) asm volatile("s_waitcnt vmcnt(%0)" ::"i"(n) : "memory")
#define WAITLGKM0() asm volatile("s_waitcnt lgkmcnt(0)" ::: "memory")
#define BARRIER() asm volatile("s_waitcnt lgkmcnt(0)\n\ts_barrier" ::: "memory")

DI void dma(unsigned ldsaddr, unsigned voff, rsrc_t rs, unsigned soff) {
  asm volatile("s_mov_b32 m0, %0\n\ts_nop 0\n\tbuffer_load_dwordx4 %1, %2, %3 offen lds" ::"s"(ldsaddr), "v"(voff), "s"(rs), "s"(soff) : "memory");
}
DI int lane_now() { int l; asm volatile("v_mbcnt_lo_u32_b32 %0, -1, 0\n\tv_mbcnt_hi_u32_b32 %0, -1, %0" : "=v"(l)); return l; }
DI bf16x8 lds128(unsigned a) { return *(LAS bf16x8*)a; }
DI bf16x4 ldstr(unsigned a) { return __builtin_amdgcn_ds_read_tr16_b64_v4bf16((LAS bf16x4*)a); }
DI float ldsf(unsigned a) { return *(LAS float*)a; }
DI unsigned cvtpk(float lo, float hi) { f32x2 v = {lo, hi}; return __builtin_bit_cast(unsigned, __builtin_convertvector(v, bf16x2)); }
DI f32x4 mfma(bf16x8 a, bf16x8 b, f32x4 c) { return __builtin_amdgcn_mfma_f32_16x16x32_bf16(a, b, c, 0, 0, 0); }
DI float ex2(float x) { return __builtin_amdgcn_exp2f(x); }
DI unsigned off_cE(int lane) { return (unsigned)((lane & 7) ^ (((lane >> 4) & 1) << 1)) << 4; }
DI unsigned off_b(int lane) { return (lane & 15) * 128 + (((lane >> 4) ^ (((lane & 15) >> 1) & 7)) << 4); }
DI unsigned off_tr(int lane, int dt) {
  const int g = lane >> 4, qq = (lane >> 2) & 3, pp = lane & 3, f = ((qq >> 1) & 1) | ((g & 1) << 1);
  return (8 * g + qq) * 128 + ((dt ^ f) << 5) + pp * 8;
}
DI unsigned off_w(int lane, int blk) {
  const int row = lane & 15, g = lane >> 4;
  return row * 128 + (((2 * blk + (g >> 1)) ^ ((row >> 1) & 7)) << 4) + (g & 1) * 8;
}
DI float xmax4(float m) { m = fmaxf(m, __shfl_xor(m, 16)); return fmaxf(m, __shfl_xor(m, 32)); }

DI void dma_unit(unsigned ldsaddr, unsigned vE, unsigned vO, rsrc_t rs, unsigned soff, unsigned stride) {
  dma(ldsaddr, vE, rs, soff);
  dma(ldsaddr + 1024, vO, rs, soff + 8 * stride);
  dma(ldsaddr + 2048, vE, rs, soff + 16 * stride);
  dma(ldsaddr + 3072, vO, rs, soff + 24 * stride);
}
struct AF { bf16x8 f[4]; };
DI void read_af(AF& a, const unsigned (&ab)[4], unsigned off) {
#pragma unroll
  for (int dt = 0; dt < 4; ++dt) {
    bf16x4 lo = ldstr(ab[dt] + off), hi = ldstr(ab[dt] + off + 512);
    a.f[dt] = __builtin_shufflevector(lo, hi, 0, 1, 2, 3, 4, 5, 6, 7);
  }
}
DI void mma20(f32x4 (&acc)[4][5], const AF& a, const bf16x8 (&bf)[5]) {
#pragma unroll
  for (int rt = 0; rt < 5; ++rt)
#pragma unroll
    for (int dt = 0; dt < 4; ++dt) acc[dt][rt] = mfma(a.f[dt], bf[rt], acc[dt][rt]);
}
DI void read_bf(bf16x8 (&bf)[5], unsigned addr) {
#pragma unroll
  for (int rt = 0; rt < 5; ++rt) bf[rt] = lds128(addr + rt * 2048);
}
DI void zero_acc(f32x4 (&acc)[4][5]) {
#pragma unroll
  for (int dt = 0; dt < 4; ++dt)
#pragma unroll
    for (int rt = 0; rt < 5; ++rt) acc[dt][rt] = f32x4{0.f, 0.f, 0.f, 0.f};
}
DI void write_img(unsigned img, const f32x4 (&acc)[4][5], const float (&sc)[5], int lane) {
#pragma unroll
  for (int dt = 0; dt < 4; ++dt) {
    const unsigned a = img + off_w(lane, dt);
#pragma unroll
    for (int rt = 0; rt < 5; ++rt) {
      u32x2 w = {cvtpk(acc[dt][rt][0] * sc[rt], acc[dt][rt][1] * sc[rt]), cvtpk(acc[dt][rt][2] * sc[rt], acc[dt][rt][3] * sc[rt])};
      *(LAS u32x2*)(a + rt * 2048) = w;
    }
  }
}
DI float read_D(int rt, u32x4 fl, int row) {
  float d = 0.f;
#pragma unroll
  for (int w = 0; w < 4; ++w) {
    const float v = ldsf(L_DLT + (w * 80 + rt * 16 + row) * 4);
    d = fmaxf(d, ((fl[w] >> rt) & 1) ? v : 0.f);
  }
  return d;
}

constexpr unsigned long long mkord(const int (&o)[16]) { unsigned long long r = 0; for (int i = 0; i < 16; ++i) r |= (unsigned long long)o[i] << (4 * i); return r; }
constexpr int ORD0[16] = {0, 4, 1, 2, 3, 8, 5, 6, 7, 12, 9, 10, 11, 13, 14, 15};
constexpr int ORD1[16] = {1, 0, 5, 2, 3, 4, 9, 6, 7, 8, 13, 10, 11, 12, 14, 15};
constexpr int ORD2[16] = {2, 0, 1, 6, 3, 4, 5, 10, 7, 8, 9, 14, 11, 12, 13, 15};
constexpr int ORD3[16] = {3, 0, 1, 2, 7, 4, 5, 6, 11, 8, 9, 10, 15, 12, 13, 14};
using I0 = std::integral_constant<int, 0>;
using I1 = std::integral_constant<int, 1>;
using I2 = std::integral_constant<int, 2>;
using I3 = std::integral_constant<int, 3>;

__global__ __launch_bounds__(512, 2) void gqa_fused(const void* __restrict__ x, const void* __restrict__ kc, const void* __restrict__ vc,
                                                    const void* __restrict__ wq, const void* __restrict__ wo, char* __restrict__ ws) {
  const int wid = __builtin_amdgcn_readfirstlane(threadIdx.x >> 6);
  const int bid = blockIdx.x, kvh = bid & 7, q = (bid >> 3) & 3, b = bid >> 5, hg = q >> 1, ph = q & 1, h = 2 * kvh + hg;
  const unsigned rotA = (unsigned)(2 * b + ph) % 7u, rotC = (unsigned)(2 * b + ph) & 3u;
  const unsigned long long ord = q == 0 ? mkord(ORD0) : q == 1 ? mkord(ORD1) : q == 2 ? mkord(ORD2) : mkord(ORD3);
#define TILE(t) ((unsigned)(ord >> (4 * (t))) & 15u)
  const rsrc_t rsWO = __builtin_amdgcn_make_buffer_rsrc((char*)wo + (size_t)h * DH * WOROW, 0, DH * WOROW, 0x00020000);
  constexpr unsigned XS[7] = {L_P0, L_P1, L_MS, L_K1, L_K1 + 10240, L_K1 + 20480, L_P0};
  unsigned hb0, hb1;

  if (wid < 4) {
    __builtin_amdgcn_s_setprio(2);
    const int w = wid;
    const rsrc_t rsK = __builtin_amdgcn_make_buffer_rsrc((char*)kc + (size_t)(b * HKV + kvh) * SK * KVROW, 0, SK * KVROW, 0x00020000);
    const unsigned ks0 = L_K0 + w * KSLOT, ks1 = L_K1 + w * KSLOT;
    unsigned kv0, kra, pwa;
    {
      const int lane = lane_now(), row = lane & 15, g = lane >> 4;
      kv0 = (lane >> 5) * 512 + (((lane & 31) ^ (lane >> 5)) << 4);
      kra = ks0 + (row >> 1) * 1056 + (row & 1) * 512 + ((g ^ (row & 1)) << 4);
      pwa = L_P0 + off_w(lane, w);
    }
    auto dmaK = [&](unsigned slot, unsigned tile) {
      const unsigned so = tile * 32768 + w * 8192;
#pragma unroll
      for (int p = 0; p < 8; ++p) dma(slot + p * 1056, kv0, rsK, so + p * 1024);
    };
    bf16x8 kf[8];
    auto readK = [&](int sl, int ks) { return lds128(kra + sl * L_K1 + ks * 64); };
    {
      const rsrc_t rsX = __builtin_amdgcn_make_buffer_rsrc((char*)x + (size_t)(b * SQ + ph * 80) * XROW, 0, 80 * XROW, 0x00020000);
      const int lane = lane_now(), r8 = lane >> 3, ch = lane & 7;
      const unsigned xE = r8 * XROW + ((ch ^ (r8 >> 1)) << 4), xO = xE ^ 64;
      auto dmaX = [&](int s) {
        unsigned c = s + rotA; c = c >= 7 ? c - 7 : c;
#pragma unroll
        for (int i = 0; i < 5; ++i) {
          const unsigned j = 5 * w + i;
          dma(XS[s] + j * 1024, (j & 1) ? xO : xE, rsX, j * 8 * XROW + c * 128);
        }
      };
      const bool xw = w < 2;
      if (xw) { dmaX(0); dmaX(1); }
      dmaK(ks0, TILE(0));
      if (xw) { dmaX(2); dmaX(3); dmaX(4); dmaX(5); }
      WAITVM(33); BARRIER();
      WAITVM(28); BARRIER();
      if (xw) { dmaX(6); WAITVM(20); } else { WAITVM(0); }
      BARRIER();
    }
#pragma unroll
    for (int ks = 0; ks < 8; ++ks) kf[ks] = readK(0, ks);
    WAITLGKM0();
    dmaK(ks0, TILE(1));
    WAITVM(23); BARRIER();
    WAITVM(18); BARRIER();
    WAITVM(13); BARRIER();
    WAITVM(8); BARRIER();
    BARRIER();
    bf16x8 qf[5][8];
    {
      const unsigned boff = off_b(lane_now());
#pragma unroll
      for (int ks = 0; ks < 8; ++ks) {
        const unsigned img = (ks >> 1) == 3 ? L_P1 : L_K1 + (ks >> 1) * 10240;
#pragma unroll
        for (int rt = 0; rt < 5; ++rt) qf[rt][ks] = lds128(img + rt * 2048 + (boff ^ ((ks & 1) * 64)));
      }
    }
    f32x4 negm[5];
    float lsum[5];
    {
      const int lane = lane_now(), row = lane & 15, g = lane >> 4;
      f32x4 S[5];
#pragma unroll
      for (int rt = 0; rt < 5; ++rt) {
        S[rt] = f32x4{0.f, 0.f, 0.f, 0.f};
#pragma unroll
        for (int ks = 0; ks < 8; ++ks) S[rt] = mfma(kf[ks], qf[rt][ks], S[rt]);
      }
#pragma unroll
      for (int rt = 0; rt < 5; ++rt) {
        const float m = xmax4(fmaxf(fmaxf(S[rt][0], S[rt][1]), fmaxf(S[rt][2], S[rt][3])));
        if (g == 0) *(LAS float*)(L_DLT + (w * 80 + rt * 16 + row) * 4) = m;
      }
      BARRIER();
      dmaK(ks1, TILE(2));
      WAITVM(8);
#pragma unroll
      for (int ks = 0; ks < 8; ++ks) kf[ks] = readK(0, ks);
#pragma unroll
      for (int rt = 0; rt < 5; ++rt) {
        float m = ldsf(L_DLT + (rt * 16 + row) * 4);
#pragma unroll
        for (int ww = 1; ww < 4; ++ww) m = fmaxf(m, ldsf(L_DLT + (ww * 80 + rt * 16 + row) * 4));
        negm[rt] = f32x4{-m, -m, -m, -m};
        const float p0 = ex2(S[rt][0] - m), p1 = ex2(S[rt][1] - m), p2 = ex2(S[rt][2] - m), p3 = ex2(S[rt][3] - m);
        lsum[rt] = (p0 + p1) + (p2 + p3);
        *(LAS u32x2*)(pwa + rt * 2048) = u32x2{cvtpk(p0, p1), cvtpk(p2, p3)};
      }
      *(LAS unsigned*)(L_FLAG + w * 4) = 0u;
      WAITLGKM0();
      dmaK(ks0, TILE(3));
      BARRIER();
    }
    auto fixup = [&](u32x4 fl, unsigned po, f32x4& S0) {
      const int row = lane_now() & 15;
      const unsigned myfl = w == 0 ? fl[0] : w == 1 ? fl[1] : w == 2 ? fl[2] : fl[3];
#pragma unroll
      for (int rt = 0; rt < 5; ++rt) {
        const float D = read_D(rt, fl, row);
        const float own = ((myfl >> rt) & 1) ? ldsf(L_DLT + (w * 80 + rt * 16 + row) * 4) : 0.f;
        const float f = ex2(own - D);
        u32x2 pk = *(LAS u32x2*)(pwa + po + rt * 2048);
        const float a0 = __uint_as_float(pk[0] << 16) * f, a1 = __uint_as_float(pk[0] & 0xffff0000u) * f;
        const float a2 = __uint_as_float(pk[1] << 16) * f, a3 = __uint_as_float(pk[1] & 0xffff0000u) * f;
        *(LAS u32x2*)(pwa + po + rt * 2048) = u32x2{cvtpk(a0, a1), cvtpk(a2, a3)};
        lsum[rt] *= f;
        negm[rt] -= D;
        if (rt == 0) S0 -= D;
        SBAR();
      }
      BARRIER();
    };
    auto smax = [&](int rt, f32x4 S, unsigned po, unsigned& wfl) {
      float p0 = ex2(S[0]), p1 = ex2(S[1]), p2 = ex2(S[2]), p3 = ex2(S[3]);
      float ts = (p0 + p1) + (p2 + p3);
      if (__builtin_expect(__any(!(ts <= PTHR)), 0)) {
        const int lane = lane_now();
        const float m = xmax4(fmaxf(fmaxf(S[0], S[1]), fmaxf(S[2], S[3])));
        const float dl = fmaxf(0.f, ceilf(m));
        p0 = ex2(S[0] - dl); p1 = ex2(S[1] - dl); p2 = ex2(S[2] - dl); p3 = ex2(S[3] - dl);
        ts = (p0 + p1) + (p2 + p3);
        lsum[rt] *= ex2(-dl);
        if (lane < 16) *(LAS float*)(L_DLT + (w * 80 + rt * 16 + lane) * 4) = dl;
        wfl |= 1u << rt;
      }
      lsum[rt] += ts;
      *(LAS u32x2*)(pwa + po + rt * 2048) = u32x2{cvtpk(p0, p1), cvtpk(p2, p3)};
    };
    auto chain = [&](int rt) {
      f32x4 S = negm[rt];
#pragma unroll
      for (int ks = 0; ks < 8; ++ks) S = mfma(kf[ks], qf[rt][ks], S);
      return S;
    };
    auto qk_tile = [&](int t, auto par_c, auto kind_c) {
      constexpr int PAR = decltype(par_c)::value, KIND = decltype(kind_c)::value;
      constexpr unsigned po = PAR * 10240, poprev = (PAR ^ 1) * 10240;
      const unsigned nslot = PAR ? ks1 : ks0;
      const u32x4 fl = *(LAS u32x4*)(L_FLAG + (PAR ^ 1) * 16);
      f32x4 S0 = chain(0);
      const unsigned anyfl = __builtin_amdgcn_readfirstlane(fl[0] | fl[1] | fl[2] | fl[3]);
      if (__builtin_expect(anyfl != 0, 0)) fixup(fl, poprev, S0);
      unsigned wfl = 0;
      f32x4 S1 = chain(1);
      smax(0, S0, po, wfl);
      S0 = chain(2);
      smax(1, S1, po, wfl);
      S1 = chain(3);
      smax(2, S0, po, wfl);
      if constexpr (KIND != 3) {
        WAITVM(8);
        S0 = negm[4];
#pragma unroll
        for (int ks = 0; ks < 8; ++ks) { S0 = mfma(kf[ks], qf[4][ks], S0); kf[ks] = readK(PAR, ks); }
      } else {
        S0 = chain(4);
      }
      smax(3, S1, po, wfl);
      smax(4, S0, po, wfl);
      *(LAS unsigned*)(L_FLAG + PAR * 16 + w * 4) = wfl;
      WAITLGKM0();
      if constexpr (KIND == 0) dmaK(nslot, TILE(t + 3));
      if constexpr (KIND == 1 || KIND == 2) {
        const int lane = lane_now();
        const unsigned woE = (lane >> 3) * WOROW + off_cE(lane), woO = woE ^ 64;
        const unsigned c = (rotC + (KIND - 1)) & 3;
        dma_unit(nslot, woE, woO, rsWO, c * 64 * WOROW + w * 128, WOROW);
        dma_unit(nslot + 4096, woE, woO, rsWO, (c * 64 + 32) * WOROW + w * 128, WOROW);
      }
      BARRIER();
    };
    for (int t = 1; t < 13; t += 2) { qk_tile(t, I1{}, I0{}); qk_tile(t + 1, I0{}, I0{}); }
    qk_tile(13, I1{}, I1{});
    qk_tile(14, I0{}, I2{});
    qk_tile(15, I1{}, I3{});
    {
      const u32x4 fl = *(LAS u32x4*)(L_FLAG + 16);
      const unsigned anyfl = __builtin_amdgcn_readfirstlane(fl[0] | fl[1] | fl[2] | fl[3]);
      f32x4 dummy = {0.f, 0.f, 0.f, 0.f};
      if (__builtin_expect(anyfl != 0, 0)) fixup(fl, 10240, dummy);
    }
    {
      const int lane = lane_now();
#pragma unroll
      for (int rt = 0; rt < 5; ++rt) {
        float l = lsum[rt];
        l += __shfl_xor(l, 16);
        l += __shfl_xor(l, 32);
        if (lane < 16) *(LAS float*)(L_LS + (w * 80 + rt * 16 + lane) * 4) = l;
      }
    }
    BARRIER();
    hb0 = ks1; hb1 = ks0;
    __builtin_amdgcn_s_setprio(0);
  } else {
    const int c = wid - 4;
    const int lane = lane_now(), row = lane & 15, r8 = lane >> 3;
    const rsrc_t rsV = __builtin_amdgcn_make_buffer_rsrc((char*)vc + (size_t)(b * HKV + kvh) * SK * KVROW, 0, SK * KVROW, 0x00020000);
    const rsrc_t rsWQ = __builtin_amdgcn_make_buffer_rsrc((char*)wq, 0, DM * WQROW, 0x00020000);
    const unsigned ring = L_VR + c * 16384;
    const unsigned cE = off_cE(lane), boff = off_b(lane);
    const unsigned wqE = r8 * WQROW + cE, wqO = wqE ^ 64;
    const unsigned vE = r8 * KVROW + cE, vO = vE ^ 64;
    const unsigned woE = r8 * WOROW + cE, woO = woE ^ 64;
    unsigned ab[4];
#pragma unroll
    for (int dt = 0; dt < 4; ++dt) ab[dt] = ring + off_tr(lane, dt);
    auto dmaWQ = [&](int u) {
      unsigned cc = (u >> 1) + rotA; cc = cc >= 7 ? cc - 7 : cc;
      dma_unit(ring + (u & 3) * 4096, wqE, wqO, rsWQ, (cc * 64 + (u & 1) * 32) * WQROW + h * 512 + c * 128, WQROW);
    };
    auto dmaV = [&](int slot, unsigned tile, int half) { dma_unit(ring + slot * 4096, vE, vO, rsV, (tile * 64 + half * 32) * KVROW + c * 128, KVROW); };
    auto dmaWO = [&](int j) {
      const unsigned cc = (rotC + (j >> 1)) & 3;
      dma_unit(ring + ((2 + j) & 3) * 4096, woE, woO, rsWO, (cc * 64 + (j & 1) * 32) * WOROW + wid * 128, WOROW);
    };
    f32x4 acc[4][5];
    zero_acc(acc);
    AF a0, a1;
    bf16x8 b0[5], b1[5];
    dmaWQ(0); dmaWQ(1); dmaWQ(2); dmaWQ(3);
    WAITVM(12);
    read_af(a0, ab, 0);
    WAITLGKM0();
    dmaWQ(4);
#pragma unroll
    for (int s = 0; s < 7; ++s) {
      BARRIER();
      read_bf(b0, XS[s] + boff);
      read_bf(b1, XS[s] + (boff ^ 64));
      WAITVM(12);
      read_af(a1, ab, ((2 * s + 1) & 3) * 4096);
      mma20(acc, a0, b0);
      SBAR();
      WAITLGKM0();
      if (2 * s + 5 < 14) dmaWQ(2 * s + 5); else dmaV((2 * s + 5) & 3, TILE((2 * s + 5 - 14) >> 1), (2 * s + 5 - 14) & 1);
      WAITVM(12);
      read_af(a0, ab, ((2 * s + 2) & 3) * 4096);
      mma20(acc, a1, b1);
      SBAR();
      WAITLGKM0();
      if (2 * s + 6 < 14) dmaWQ(2 * s + 6); else dmaV((2 * s + 6) & 3, TILE((2 * s + 6 - 14) >> 1), (2 * s + 6 - 14) & 1);
    }
    {
      const float sc[5] = {QSCALE, QSCALE, QSCALE, QSCALE, QSCALE};
      write_img(c == 3 ? L_P1 : L_K1 + c * 10240, acc, sc, lane);
    }
    zero_acc(acc);
    BARRIER();
    BARRIER();
    auto pv_tile = [&](int t, auto par_c, auto kind_c) {
      constexpr int PAR = decltype(par_c)::value, KIND = decltype(kind_c)::value;
      const unsigned pb = PAR ? L_P1 : L_P0;
      constexpr int s0 = PAR ? 0 : 2;
      BARRIER();
      u32x4 fl = *(LAS u32x4*)(L_FLAG + PAR * 16);
      const unsigned anyfl = __builtin_amdgcn_readfirstlane(fl[0] | fl[1] | fl[2] | fl[3]);
      if (__builtin_expect(anyfl != 0, 0)) {
#pragma unroll
        for (int rt = 0; rt < 5; ++rt) {
          const float f = ex2(-read_D(rt, fl, row));
#pragma unroll
          for (int dt = 0; dt < 4; ++dt) acc[dt][rt] *= f;
        }
        BARRIER();
      }
      read_bf(b0, pb + boff);
      read_bf(b1, pb + (boff ^ 64));
      if constexpr (KIND == 2) { if (c == 3) WAITVM(8); else WAITVM(12); }
      else if constexpr (KIND == 3) { if (c == 3) WAITVM(0); else WAITVM(12); }
      else WAITVM(12);
      read_af(a1, ab, (s0 + 1) * 4096);
      mma20(acc, a0, b0);
      SBAR();
      WAITLGKM0();
      if constexpr (KIND <= 1) dmaV(s0 + 1, TILE(t + 2), 1);
      else if (c != 3) dmaWO(KIND == 2 ? 1 : 3);
      if constexpr (KIND == 3) BARRIER();
      if constexpr (KIND != 3) {
        if constexpr (KIND == 2) { if (c == 3) WAITVM(4); else WAITVM(12); }
        else WAITVM(12);
        read_af(a0, ab, ((s0 + 2) & 3) * 4096);
      }
      mma20(acc, a1, b1);
      SBAR();
      if constexpr (KIND != 3) {
        WAITLGKM0();
        if constexpr (KIND == 0) dmaV((s0 + 2) & 3, TILE(t + 3), 0);
        else if (c != 3) dmaWO(KIND == 1 ? 0 : 2);
      }
    };
    for (int t = 0; t < 12; t += 2) { pv_tile(t, I0{}, I0{}); pv_tile(t + 1, I1{}, I0{}); }
    pv_tile(12, I0{}, I0{});
    pv_tile(13, I1{}, I1{});
    pv_tile(14, I0{}, I2{});
    pv_tile(15, I1{}, I3{});
    float inv[5];
#pragma unroll
    for (int rt = 0; rt < 5; ++rt) {
      float l = 0.f;
#pragma unroll
      for (int ww = 0; ww < 4; ++ww) l += ldsf(L_LS + (ww * 80 + rt * 16 + row) * 4);
      inv[rt] = 1.f / l;
    }
    write_img(c == 0 ? L_P0 : c == 1 ? L_P1 : c == 2 ? L_MS : L_VR + 3 * 16384, acc, inv, lane);
    hb0 = ring + 8192; hb1 = ring;
  }

  if (wid < 7) {
    const int lane = lane_now();
    const unsigned boff = off_b(lane);
    const unsigned woE = (lane >> 3) * WOROW + off_cE(lane), woO = woE ^ 64;
    unsigned ab0[4], ab1[4];
#pragma unroll
    for (int dt = 0; dt < 4; ++dt) { ab0[dt] = hb0 + off_tr(lane, dt); ab1[dt] = hb1 + off_tr(lane, dt); }
    auto dmaWO = [&](int j) {
      const unsigned cc = (rotC + (j >> 1)) & 3;
      dma_unit((((j >> 1) & 1) ? hb1 : hb0) + (j & 1) * 4096, woE, woO, rsWO, (cc * 64 + (j & 1) * 32) * WOROW + wid * 128, WOROW);
    };
    f32x4 acc[4][5];
    zero_acc(acc);
    AF a0, a1;
    bf16x8 b0[5], b1[5];
    WAITVM(12);
    read_af(a0, ab0, 0);
    WAITLGKM0();
    dmaWO(4);
    BARRIER();
#pragma unroll
    for (int s = 0; s < 4; ++s) {
      const unsigned cc = (rotC + s) & 3;
      const unsigned img = cc == 0 ? L_P0 : cc == 1 ? L_P1 : cc == 2 ? L_MS : L_VR + 3 * 16384;
      read_bf(b0, img + boff);
      read_bf(b1, img + (boff ^ 64));
      if (s < 2) WAITVM(12); else if (s == 2) WAITVM(8); else WAITVM(0);
      read_af(a1, (s & 1) ? ab1 : ab0, 4096);
      mma20(acc, a0, b0);
      SBAR();
      if (s < 2) { WAITLGKM0(); dmaWO(2 * s + 5); }
      if (s < 3) {
        if (s < 2) WAITVM(12); else WAITVM(4);
        read_af(a0, (s & 1) ? ab0 : ab1, 0);
      }
      mma20(acc, a1, b1);
      SBAR();
      if (s < 1) { WAITLGKM0(); dmaWO(2 * s + 6); }
    }
    char* slab = ws + (size_t)((b * 2 + ph) * 16 + h) * SLAB + (size_t)wid * 10240 + lane * 16;
#pragma unroll
    for (int rt = 0; rt < 5; ++rt)
#pragma unroll
      for (int np = 0; np < 2; ++np) {
        const f32x4 u = acc[2 * np][rt], v = acc[2 * np + 1][rt];
        *(u32x4*)(slab + (rt * 2 + np) * 1024) = u32x4{cvtpk(u[0], u[1]), cvtpk(u[2], u[3]), cvtpk(v[0], v[1]), cvtpk(v[2], v[3])};
      }
  } else {
    BARRIER();
  }
}

__global__ __launch_bounds__(256) void gqa_reduce(const char* __restrict__ ws, char* __restrict__ out) {
  const int id = blockIdx.x * 256 + threadIdx.x;
  const int grp = id / 4480, ci = id % 4480;
  const char* src = ws + (size_t)grp * 16 * SLAB + ci * 16;
  u32x4 v[16];
#pragma unroll
  for (int hh = 0; hh < 16; ++hh) v[hh] = *(const u32x4*)(src + (size_t)hh * SLAB);
  float s[8] = {0.f, 0.f, 0.f, 0.f, 0.f, 0.f, 0.f, 0.f};
#pragma unroll
  for (int hh = 0; hh < 16; ++hh)
#pragma unroll
    for (int i = 0; i < 4; ++i) { s[2 * i] += __uint_as_float(v[hh][i] << 16); s[2 * i + 1] += __uint_as_float(v[hh][i] & 0xffff0000u); }
  const int piece = ci >> 6, lane = ci & 63, sl = piece / 10, rt = (piece % 10) >> 1, np = piece & 1;
  const int row = 16 * rt + (lane & 15), n0 = 64 * sl + 32 * np + 4 * (lane >> 4);
  char* dst = out + ((size_t)(grp * 80 + row) * DM + n0) * 2;
  *(u32x2*)dst = u32x2{cvtpk(s[0], s[1]), cvtpk(s[2], s[3])};
  *(u32x2*)(dst + 32) = u32x2{cvtpk(s[4], s[5]), cvtpk(s[6], s[7])};
}

extern "C" void kernel_launch(void* const* d_in, const int* in_sizes, int n_in, void* d_out, int out_size, void* d_ws, size_t ws_size,
                              hipStream_t stream) {
  (void)in_sizes; (void)n_in; (void)out_size; (void)ws_size;
  (void)hipFuncSetAttribute((const void*)gqa_fused, hipFuncAttributeMaxDynamicSharedMemorySize, L_TOTAL);
  gqa_fused<<<256, 512, L_TOTAL, stream>>>(d_in[0], d_in[1], d_in[2], d_in[3], d_in[4], (char*)d_ws);
  gqa_reduce<<<280, 256, 0, stream>>>((const char*)d_ws, (char*)d_out);
}
```

```cpp
#include <hip/hip_runtime.h>
#include <hip/hip_bf16.h>
#include <type_traits>
#define PP 1
#define QD 1
#define PRQ 2
#define PRP 0
#define ABL 4096
#pragma clang diagnostic ignored "-Wint-to-pointer-cast"

typedef __bf16 bf16x8 __attribute__((ext_vector_type(8)));
typedef __bf16 bf16x4 __attribute__((ext_vector_type(4)));
typedef __bf16 bf16x2 __attribute__((ext_vector_type(2)));
typedef float f32x4 __attribute__((ext_vector_type(4)));
typedef float f32x2 __attribute__((ext_vector_type(2)));
typedef unsigned u32x4 __attribute__((ext_vector_type(4)));
typedef unsigned u32x2 __attribute__((ext_vector_type(2)));
#define LAS __attribute__((address_space(3)))
#define DI __device__ __forceinline__
typedef __amdgpu_buffer_rsrc_t rsrc_t;

constexpr int NB = 8, SQ = 160, DM = 448, HQ = 16, HKV = 8, SK = 1024, DH = 256;
constexpr unsigned XROW = DM * 2, WQROW = HQ * DH * 2, KVROW = DH * 2, WOROW = DM * 2;
constexpr unsigned SLAB = 80 * DM * 2;
constexpr unsigned KSLOT = 8448;
constexpr unsigned L_K0 = 0, L_K1 = 4 * KSLOT, L_VR = 8 * KSLOT, L_P0 = L_VR + 65536, L_P1 = L_P0 + 10240, L_MS = L_P1 + 10240, L_TOTAL = L_MS + 10240;
static_assert(L_TOTAL == 163840, "LDS");
constexpr unsigned L_FLAG = L_MS;
constexpr unsigned L_DLT = L_MS + 128;
constexpr unsigned L_OI3 = L_VR + 3 * 16384;
constexpr float QSCALE = 0.0625f * 1.4426950408889634f;
#define PTHR 65536.f
#define PLOW (1.f / 65536.f)

#define STAMP(k)
#define ABL_KSZ(x) ((ABL & 1) ? 0 : (x))
#define ABL_VSZ(x) ((ABL & 1024) ? 0 : (x))
#define SBAR() __builtin_amdgcn_sched_barrier(0)
#define TR(k)
#define TRSAVE(cond)
#define PRIO_HI() __builtin_amdgcn_s_setprio(1)
#define PRIO_LO() __builtin_amdgcn_s_setprio(0)
#define WAITVM(n) asm volatile("s_waitcnt vmcnt(%0)" ::"i"(n) : "memory")
#define WAITLGKM0() do { asm volatile("" ::: "memory"); __builtin_amdgcn_s_waitcnt(0xC07F); asm volatile("" ::: "memory"); } while (0)
#define BARRIER() do { WAITLGKM0(); asm volatile("s_barrier" ::: "memory"); } while (0)

DI void dma(unsigned ldsaddr, unsigned voff, rsrc_t rs, unsigned soff) {
  asm volatile("s_mov_b32 m0, %0\n\ts_nop 0\n\tbuffer_load_dwordx4 %1, %2, %3 offen lds" ::"s"(ldsaddr), "v"(voff), "s"(rs), "s"(soff) : "memory");
}
DI int lane_now() { unsigned z = 0; asm volatile("" : "+v"(z)); return __builtin_amdgcn_mbcnt_hi(~0u, __builtin_amdgcn_mbcnt_lo(~0u, z)); }
DI bf16x8 lds128(unsigned a) { return *(LAS bf16x8*)a; }
DI bf16x4 ldstr(unsigned a) { return __builtin_amdgcn_ds_read_tr16_b64_v4bf16((LAS bf16x4*)a); }
DI float ldsf(unsigned a) { return *(LAS float*)a; }
DI unsigned cvtpk(float lo, float hi) { f32x2 v = {lo, hi}; return __builtin_bit_cast(unsigned, __builtin_convertvector(v, bf16x2)); }
DI f32x4 mfma(bf16x8 a, bf16x8 b, f32x4 c) { return __builtin_amdgcn_mfma_f32_16x16x32_bf16(a, b, c, 0, 0, 0); }
DI void mfma_acc(f32x4& c, bf16x8 a, bf16x8 b) { asm volatile("v_mfma_f32_16x16x32_bf16 %0, %1, %2, %0" : "+v"(c) : "v"(a), "v"(b)); }
DI void mfma_init(f32x4& d, bf16x8 a, bf16x8 b, f32x4 c) { asm volatile("v_mfma_f32_16x16x32_bf16 %0, %1, %2, %3" : "=&v"(d) : "v"(a), "v"(b), "v"(c)); }
#define MFMA_DRAIN() asm volatile("s_nop 7\n\ts_nop 1" ::: "memory")
DI float ex2(float x) { return __builtin_amdgcn_exp2f(x); }
DI unsigned off_cE(int lane) { return (unsigned)((lane & 7) ^ (((lane >> 4) & 1) << 1)) << 4; }
DI unsigned off_b(int lane) { return (lane & 15) * 128 + (((lane >> 4) ^ (((lane & 15) >> 1) & 7)) << 4); }
DI unsigned off_tr(int lane, int dt) {
  const int g = lane >> 4, qq = (lane >> 2) & 3, pp = lane & 3, f = ((qq >> 1) & 1) | ((g & 1) << 1);
  return (8 * g + qq) * 128 + ((dt ^ f) << 5) + pp * 8;
}
DI unsigned off_w(int lane, int blk) {
  const int row = lane & 15, g = lane >> 4;
  return (blk >> 1) * 5120 + (g & 1) * 512 + ((2 * (blk & 1) + (g >> 1)) * 16 + row) * 8;
}
DI bf16x8 ldsplit(unsigned a) {
  const bf16x4 lo = *(LAS bf16x4*)a, hi = *(LAS bf16x4*)(a + 512);
  return __builtin_shufflevector(lo, hi, 0, 1, 2, 3, 4, 5, 6, 7);
}
DI float xmax4(float m) { m = fmaxf(m, __shfl_xor(m, 16)); return fmaxf(m, __shfl_xor(m, 32)); }

template <int IMM> DI void dma0(unsigned ldsaddr, unsigned voff, rsrc_t rs, unsigned soff) {
  asm volatile("s_mov_b32 m0, %0\n\ts_nop 0\n\tbuffer_load_dwordx4 %1, %2, %3 offen offset:%4 lds" ::"s"(ldsaddr), "v"(voff), "s"(rs), "s"(soff), "i"(IMM) : "memory");
}
template <int IMM> DI void dmaN(unsigned voff, rsrc_t rs, unsigned soff) {
  asm volatile("buffer_load_dwordx4 %0, %1, %2 offen offset:%3 lds" ::"v"(voff), "s"(rs), "s"(soff), "i"(IMM) : "memory");
}
DI void unit_voff(unsigned (&v)[4], int lane, unsigned stride) {
  const unsigned e = (lane >> 3) * stride + off_cE(lane);
#pragma unroll
  for (int p = 0; p < 4; ++p) v[p] = ((p & 1) ? e ^ 64 : e) + p * (8 * stride - 1024);
}
DI void dma_unit(unsigned ldsaddr, const unsigned (&v)[4], rsrc_t rs, unsigned soff) {
  dma0<0>(ldsaddr, v[0], rs, soff); dmaN<1024>(v[1], rs, soff); dmaN<2048>(v[2], rs, soff); dmaN<3072>(v[3], rs, soff);
}
struct OPS { bf16x8 a[4], b[5]; };
template <bool MMA, bool READ, bool DMA, bool SPLIT = true>
DI void step(f32x4 (&acc)[4][5], const OPS& cur, OPS& nxt, const unsigned (&toff)[4], unsigned aslot, unsigned baddr,
             unsigned dslot, const unsigned (&dv)[4], rsrc_t rs, unsigned soff) {
  bf16x4 lo, hi;
  unsigned aa;
#pragma unroll
  for (int i = 0; i < 20; ++i) {
    if (MMA && !((ABL & 8) && DMA)) acc[i & 3][i >> 2] = mfma(cur.a[i & 3], cur.b[i >> 2], acc[i & 3][i >> 2]);
    if (READ && !((ABL & 4) && DMA)) {
      if (i == 0) nxt.b[0] = SPLIT ? ldsplit(baddr) : lds128(baddr);
      if (i >= 1 && i <= 8) {
        const int dt = (i - 1) >> 1;
        if ((i - 1) & 1) { hi = ldstr(aa + 512); nxt.a[dt] = __builtin_shufflevector(lo, hi, 0, 1, 2, 3, 4, 5, 6, 7); }
        else { aa = aslot + toff[dt]; lo = ldstr(aa); }
      }
      if (i >= 9 && i <= 12) nxt.b[i - 8] = SPLIT ? ldsplit(baddr + (i - 8) * 1024) : lds128(baddr + (i - 8) * 2048);
    }
    if (DMA && !(ABL & 2)) {
      if (i == 5) dma0<0>(dslot, dv[0], rs, soff);
      if (i == 9) dmaN<1024>(dv[1], rs, soff);
      if (i == 13) dmaN<2048>(dv[2], rs, soff);
      if (i == 17) dmaN<3072>(dv[3], rs, soff);
    }
    SBAR();
  }
}
DI void read_b(OPS& o, unsigned addr) {
#pragma unroll
  for (int rt = 0; rt < 5; ++rt) o.b[rt] = ldsplit(addr + rt * 1024);
}
DI void zero_acc(f32x4 (&acc)[4][5]) {
#pragma unroll
  for (int dt = 0; dt < 4; ++dt)
#pragma unroll
    for (int rt = 0; rt < 5; ++rt) acc[dt][rt] = f32x4{0.f, 0.f, 0.f, 0.f};
}
DI void write_img(unsigned img, const f32x4 (&acc)[4][5], const float (&sc)[5], int lane) {
#pragma unroll
  for (int dt = 0; dt < 4; ++dt) {
    const unsigned a = img + off_w(lane, dt);
#pragma unroll
    for (int rt = 0; rt < 5; ++rt) {
      u32x2 w = {cvtpk(acc[dt][rt][0] * sc[rt], acc[dt][rt][1] * sc[rt]), cvtpk(acc[dt][rt][2] * sc[rt], acc[dt][rt][3] * sc[rt])};
      *(LAS u32x2*)(a + rt * 1024) = w;
    }
  }
}
DI float read_D(int rt, unsigned fl, int row) {
  float d[4];
#pragma unroll
  for (int w = 0; w < 4; ++w) {
    const float v = ldsf(L_DLT + (w * 80 + rt * 16 + row) * 4);
    d[w] = ((fl >> (8 * w + rt)) & 1) ? v : 0.f;
  }
  return fmaxf(fmaxf(d[0], d[1]), fmaxf(d[2], d[3]));
}
DI float fadd_s(float a, float b) { float r = a + b; asm("" : "+v"(r)); return r; }
DI unsigned ls_addr(int w, int rt, int row) { return (rt < 4 ? L_K0 + rt * 64 : L_K1) + w * KSLOT + 8192 + row * 4; }

constexpr unsigned long long mkord(const int (&o)[16]) { unsigned long long r = 0; for (int i = 0; i < 16; ++i) r |= (unsigned long long)o[i] << (4 * i); return r; }
constexpr int ORD0[16] = {0, 4, 1, 2, 3, 8, 5, 6, 7, 12, 9, 10, 11, 13, 14, 15};
constexpr int ORD1[16] = {1, 0, 5, 2, 3, 4, 9, 6, 7, 8, 13, 10, 11, 12, 14, 15};
constexpr int ORD2[16] = {2, 0, 1, 6, 3, 4, 5, 10, 7, 8, 9, 14, 11, 12, 13, 15};
constexpr int ORD3[16] = {3, 0, 1, 2, 7, 4, 5, 6, 11, 8, 9, 10, 15, 12, 13, 14};

__global__ __launch_bounds__(512, 2) void gqa_fused(const void* __restrict__ x, const void* __restrict__ kc, const void* __restrict__ vc,
                                                    const void* __restrict__ wq, const void* __restrict__ wo, char* __restrict__ ws) {
  STAMP(0);
  const int wid = __builtin_amdgcn_readfirstlane(threadIdx.x >> 6);
  const int bid = blockIdx.x, kvh = bid & 7, q = (bid >> 3) & 3, b = bid >> 5, hg = q >> 1, ph = q & 1, h = 2 * kvh + hg;
  const unsigned rotA = (unsigned)(2 * b + ph) % 7u, rotC = (unsigned)(2 * b + ph) & 3u;
  const unsigned long long ord = q == 0 ? mkord(ORD0) : q == 1 ? mkord(ORD1) : q == 2 ? mkord(ORD2) : mkord(ORD3);
#define TILE(t) ((ABL & 4096) ? (unsigned)(t) : ((unsigned)(ord >> (4 * (t))) & 15u))
  const rsrc_t rsWO = __builtin_amdgcn_make_buffer_rsrc((char*)wo + (size_t)h * DH * WOROW, 0, wid == 7 ? 0 : DH * WOROW, 0x00020000);
  const rsrc_t rsV = __builtin_amdgcn_make_buffer_rsrc((char*)vc + (size_t)(((ABL & 8192) ? 0 : b) * HKV + kvh) * SK * KVROW, 0, ABL_VSZ(SK * KVROW), 0x00020000);
  const int w = wid & 3;
  const unsigned ks0 = L_K0 + w * KSLOT, ks1 = L_K1 + w * KSLOT, vra = L_VR + w * 16384, vrb = vra + 8192;
  unsigned hb0, hb1;

  if (wid < 4) {
    const rsrc_t rsK = __builtin_amdgcn_make_buffer_rsrc((char*)kc + (size_t)(((ABL & 8192) ? 0 : b) * HKV + kvh) * SK * KVROW - 128, 0, ABL_KSZ(SK * KVROW + 128), 0x00020000);
    unsigned kv0, kra, pwa;
    {
      const int lane = lane_now(), row = lane & 15, g = lane >> 4;
      kv0 = (lane >> 5) * 512 + (((lane & 31) ^ (lane >> 5)) << 4);
      kra = ks0 + (row >> 1) * 1056 + (row & 1) * 512 + ((g ^ (row & 1)) << 4);
      pwa = L_P0 + off_w(lane, w);
    }
#define DMAK(P, slot, so)                                                                     \
  do {                                                                                        \
    if (((P) & 3) == 0) dma0<0>((slot) + ((P) >> 2) * 4224, kv0, rsK, (so) + ((P) >> 2) * 4096); \
    else dmaN<((P) & 3) * 1056>(kv0, rsK, (so) + ((P) >> 2) * 4096 - ((P) & 3) * 32);           \
  } while (0)
    auto dmaK = [&](unsigned slot, unsigned tile) __attribute__((always_inline)) {
      const unsigned so = tile * 32768 + w * 8192 + 128;
      DMAK(0, slot, so); DMAK(1, slot, so); DMAK(2, slot, so); DMAK(3, slot, so);
      DMAK(4, slot, so); DMAK(5, slot, so); DMAK(6, slot, so); DMAK(7, slot, so);
    };
    bf16x8 kf[8];
    {
      const rsrc_t rsX = __builtin_amdgcn_make_buffer_rsrc((char*)x + (size_t)(b * SQ + ph * 80) * XROW, 0, 80 * XROW, 0x00020000);
      const rsrc_t rsWQ = __builtin_amdgcn_make_buffer_rsrc((char*)wq, 0, DM * WQROW, 0x00020000);
      const int lane = lane_now(), r8 = lane >> 3, ch = lane & 7;
      unsigned wv[4], xv[3];
      unit_voff(wv, lane, WQROW);
#pragma unroll
      for (int i = 0; i < 3; ++i) { const int o = i == 2 ? 12 : 8 * i, r = 20 * w + o + r8; xv[i] = r * XROW + ((ch ^ ((r >> 1) & 7)) << 4) - o * 128; }
      unsigned cc = rotA, as0 = vrb, as1 = vra, as2 = ks1, xs0 = L_P0, xs1 = L_P1, xs2 = L_MS;
      auto issue = [&]() __attribute__((always_inline)) {
        dma0<0>(xs0 + 20 * w * 128, xv[0], rsX, cc * 128); dmaN<1024>(xv[1], rsX, cc * 128); dmaN<1536>(xv[2], rsX, cc * 128);
        const unsigned so = cc * 64 * WQROW + h * 512 + w * 128;
        dma_unit(as0, wv, rsWQ, so);
        dma_unit(as0 + 4096, wv, rsWQ, so + 32 * WQROW);
        cc = cc == 6 ? 0 : cc + 1;
        unsigned t0 = as0; as0 = as1; as1 = as2; as2 = t0;
        t0 = xs0; xs0 = xs1; xs1 = xs2; xs2 = t0;
      };
      issue();
#pragma unroll 1
      for (int s = 0; s < 6; ++s) {
        issue();
        WAITVM(11);
        BARRIER();
        if (s == 0) STAMP(1);
      }
      dmaK(ks0, TILE(0));
      unit_voff(wv, lane, KVROW);
      dma_unit(vra, wv, rsV, TILE(0) * 32768 + w * 128);
      dma_unit(vra + 4096, wv, rsV, TILE(0) * 32768 + 16384 + w * 128);
      WAITVM(16);
      BARRIER();
    }
    WAITVM(8);
#pragma unroll
    for (int ks = 0; ks < 8; ++ks) kf[ks] = lds128(kra + ks * 64);
    if (w == 0) { const int lane = lane_now(); if (lane < 32) *(LAS unsigned*)(L_FLAG + lane * 4) = 0u; }
    WAITLGKM0();
    dmaK(ks0, TILE(1));
    BARRIER();
    STAMP(2);
    bf16x8 qf[5][8];
    {
      const unsigned l8 = lane_now() * 8;
#pragma unroll
      for (int rt = 0; rt < 5; ++rt)
#pragma unroll
        for (int ks = 0; ks < 8; ++ks) {
          const unsigned img = (ks >> 1) == 3 ? L_P1 : L_K1 + (ks >> 1) * 10240;
          qf[rt][ks] = ldsplit(img + (ks & 1) * 5120 + rt * 1024 + l8);
        }
    }
    f32x4 negm[5];
    float lsum[5];
#pragma unroll
    for (int rt = 0; rt < 5; ++rt) { negm[rt] = f32x4{0.f, 0.f, 0.f, 0.f}; lsum[rt] = 0.f; }
    f32x4 S0 = negm[0];
#pragma unroll
    for (int ks = 0; ks < 8; ++ks) S0 = mfma(kf[ks], qf[0][ks], S0);
    WAITVM(8);
    BARRIER();
    auto fixup = [&](unsigned fl, unsigned pprev, f32x4& Sa, f32x4& Sb) __attribute__((always_inline)) {
      const int row = lane_now() & 15;
#pragma unroll
      for (int rt = 0; rt < 5; ++rt) {
        const float D = read_D(rt, fl, row);
        const float own = ((fl >> (8 * w + rt)) & 1) ? ldsf(L_DLT + (w * 80 + rt * 16 + row) * 4) : 0.f;
        const float f = ex2(own - D);
        u32x2 pk = *(LAS u32x2*)(pprev + rt * 1024);
        const float a0 = __uint_as_float(pk[0] << 16) * f, a1 = __uint_as_float(pk[0] & 0xffff0000u) * f;
        const float a2 = __uint_as_float(pk[1] << 16) * f, a3 = __uint_as_float(pk[1] & 0xffff0000u) * f;
        *(LAS u32x2*)(pprev + rt * 1024) = u32x2{cvtpk(a0, a1), cvtpk(a2, a3)};
        lsum[rt] *= f;
        negm[rt] -= D;
        if (rt == 0) Sa -= D;
        if (rt == 1) Sb -= D;
        SBAR();
      }
      BARRIER();
    };
    unsigned dslot = ks1;
    const unsigned pwsum = 2 * pwa + 10240, krsum = 2 * kra + L_K1;
    auto qk_tile = [&](int t, auto wo_c) __attribute__((always_inline)) {
      constexpr bool WO = decltype(wo_c)::value;
      unsigned so, wv[4];
      if constexpr (WO) { unit_voff(wv, lane_now(), WOROW); so = ((rotC + t - 14) & 3) * 64 * WOROW + w * 128; }
      else so = TILE(t + 2) * 32768 + w * 8192 + 128;
      unsigned fl = *(LAS unsigned*)(L_FLAG + t * 4);
      f32x4 S1;
      float p0, p1, p2, p3, ts, tm;
      u32x2 pk;
#define QDMA(P)                                                                                                   \
  do {                                                                                                            \
    if constexpr (WO) {                                                                                           \
      if (((P) & 3) == 0) dma0<0>(dslot + ((P) >> 2) * 4096, wv[0], rsWO, so + ((P) >> 2) * 32 * WOROW);           \
      else dmaN<((P) & 3) * 1024>(wv[(P) & 3], rsWO, so + ((P) >> 2) * 32 * WOROW);                                \
    } else if (!(ABL & 2048)) DMAK(P, dslot, so);                                                                                    \
  } while (0)
#define QM(i, RN, SN)                                                                                              \
  if (ABL & 16) { SN = negm[RN]; asm volatile("" : "+v"(SN) : "v"(kf[i]), "v"(qf[RN][i])); }                       \
  else if ((i) == 0) mfma_init(SN, kf[i], qf[RN][i], negm[RN]); else mfma_acc(SN, kf[i], qf[RN][i]);
#define QKBLOCK(RC, SC, RN, SN)                                                                                    \
  TR(2 * RC); QM(0, RN, SN) SBAR(); PRIO_HI(); p0 = ex2(SC[0]); SBAR();                                                        \
  QM(1, RN, SN) SBAR(); p1 = ex2(SC[1]); SBAR();                                                                   \
  QM(2, RN, SN) SBAR(); p2 = ex2(SC[2]); SBAR();                                                                   \
  QM(3, RN, SN) SBAR(); p3 = ex2(SC[3]); SBAR();                                                                   \
  QM(4, RN, SN) SBAR(); ts = fadd_s(p0, p1); pk[0] = cvtpk(p0, p1); SBAR();                                        \
  QM(5, RN, SN) SBAR(); p0 = fadd_s(p2, p3); pk[1] = cvtpk(p2, p3); SBAR();                                        \
  QM(6, RN, SN) SBAR(); ts = fadd_s(ts, p0); if (RC == 0) fl = __builtin_amdgcn_readfirstlane(fl); SBAR();         \
  QM(7, RN, SN) SBAR(); PRIO_LO(); TR(2 * RC + 1);                                                                                \
  if (RC == 0) {                                                                                                   \
    if (__builtin_expect(fl != 0, 0)) {                                                                            \
      MFMA_DRAIN();                                                                                                \
      fixup(fl, pwsum - pwa, SC, SN);                                                                              \
      p0 = ex2(SC[0]); p1 = ex2(SC[1]); p2 = ex2(SC[2]); p3 = ex2(SC[3]);                                          \
      ts = (p0 + p1) + (p2 + p3);                                                                                  \
      pk = u32x2{cvtpk(p0, p1), cvtpk(p2, p3)};                                                                    \
    }                                                                                                              \
    SBAR();                                                                                                        \
  }                                                                                                                \
  if (RC == 3) {                                                                                                   \
    TR(13); WAITVM(6); TR(14);                                                                                     \
    _Pragma("unroll") for (int ks = 0; ks < 8; ++ks) kf[ks] = lds128(kra + ks * 64);                               \
    SBAR();                                                                                                        \
  }                                                                                                                \
  lsum[RC] = fadd_s(lsum[RC], ts); tm = RC == 0 ? ts : fadd_s(tm, ts);                                             \
  *(LAS u32x2*)(pwa + RC * 1024) = pk; SBAR();
      SBAR();
      QKBLOCK(0, S0, 1, S1)
      QDMA(0); QDMA(1); SBAR();
      QKBLOCK(1, S1, 2, S0)
      QDMA(2); QDMA(3); SBAR();
      QKBLOCK(2, S0, 3, S1)
      QDMA(4); QDMA(5); SBAR();
      QKBLOCK(3, S1, 4, S0)
      QKBLOCK(4, S0, 0, S1)
      QDMA(6); QDMA(7); SBAR();
      S0 = S1;
#undef QKBLOCK
#undef QM
#undef QDMA
      bool bad = !(tm <= PTHR);
      if (t == 0) bad |= !(fminf(fminf(fminf(lsum[0], lsum[1]), fminf(lsum[2], lsum[3])), lsum[4]) >= PLOW);
      if (!(ABL & 128)) if (__builtin_expect(__any(bad), 0)) {
        const int lane = lane_now();
#pragma unroll
        for (int ks = 0; ks < 8; ++ks)
          kf[ks] = __builtin_bit_cast(bf16x8, __builtin_amdgcn_raw_buffer_load_b128(rsK, (lane & 15) * 512 + (lane >> 4) * 16 + ks * 64, TILE(t) * 32768 + w * 8192 + 128, 0));
#pragma unroll
        for (int rt = 0; rt < 5; ++rt) {
          f32x4 S = negm[rt];
#pragma unroll
          for (int ks = 0; ks < 8; ++ks) S = mfma(kf[ks], qf[rt][ks], S);
          float dl = ceilf(xmax4(fmaxf(fmaxf(S[0], S[1]), fmaxf(S[2], S[3]))));
          if (t > 0) dl = fmaxf(dl, 0.f);
          p0 = ex2(S[0] - dl); p1 = ex2(S[1] - dl); p2 = ex2(S[2] - dl); p3 = ex2(S[3] - dl);
          lsum[rt] = (t > 0 && lsum[rt] <= 1.0e18f) ? lsum[rt] * ex2(-dl) : (p0 + p1) + (p2 + p3);
          *(LAS u32x2*)(pwa + rt * 1024) = u32x2{cvtpk(p0, p1), cvtpk(p2, p3)};
          if (lane < 16) *(LAS float*)(L_DLT + (w * 80 + rt * 16 + lane) * 4) = dl;
          SBAR();
        }
#pragma unroll
        for (int ks = 0; ks < 8; ++ks) kf[ks] = lds128(kra + ks * 64);
        *(LAS unsigned char*)(L_FLAG + (t + 1) * 4 + w) = 31;
      }
      kra = krsum - kra;
      pwa = pwsum - pwa;
      dslot = ks0 + ks1 - dslot;
      TR(10);
      WAITLGKM0();
      TR(11);
      BARRIER();
      TR(12);
      TRSAVE(t == 7);
    };
#pragma unroll 1
    for (int t = 0; t < 14; ++t) {
      qk_tile(t, std::false_type{});
    }
#pragma unroll 1
    for (int t = 14; t < 16; ++t) qk_tile(t, std::true_type{});
    {
      const unsigned fl = __builtin_amdgcn_readfirstlane(*(LAS unsigned*)(L_FLAG + 64));
      f32x4 dummy = {0.f, 0.f, 0.f, 0.f};
      if (__builtin_expect(fl != 0, 0)) fixup(fl, pwsum - pwa, dummy, dummy);
    }
    {
      const int lane = lane_now();
#pragma unroll
      for (int rt = 0; rt < 5; ++rt) {
        float l = lsum[rt];
        l += __shfl_xor(l, 16);
        l += __shfl_xor(l, 32);
        if (lane < 16) *(LAS float*)ls_addr(w, rt, lane) = l;
      }
    }
    BARRIER();
    STAMP(5);
    hb0 = ks1; hb1 = ks0;
  } else {
    const int lane = lane_now(), row = lane & 15;
    const unsigned xoff = off_b(lane), xoff1 = xoff ^ 64, boff = lane * 8, boff1 = boff + 5120;
    unsigned toff[4], dv[4];
#pragma unroll
    for (int dt = 0; dt < 4; ++dt) toff[dt] = off_tr(lane, dt);
    unit_voff(dv, lane, KVROW);
    f32x4 acc[4][5];
    zero_acc(acc);
    OPS o0, o1;
    {
      unsigned as0 = vrb, as1 = vra, as2 = ks1, xs0 = L_P0, xs1 = L_P1, xs2 = L_MS;
      BARRIER();
      STAMP(1);
      step<false, true, false, false>(acc, o1, o0, toff, as0, xs0 + xoff, 0, dv, rsV, 0);
#pragma unroll 1
      for (int s = 0; s < 6; ++s) {
        step<true, true, false, false>(acc, o0, o1, toff, as0 + 4096, xs0 + xoff1, 0, dv, rsV, 0);
        unsigned t0 = as0; as0 = as1; as1 = as2; as2 = t0;
        t0 = xs0; xs0 = xs1; xs1 = xs2; xs2 = t0;
        BARRIER();
        step<true, true, false, false>(acc, o1, o0, toff, as0, xs0 + xoff, 0, dv, rsV, 0);
      }
      step<true, true, false, false>(acc, o0, o1, toff, as0 + 4096, xs0 + xoff1, 0, dv, rsV, 0);
      step<true, false, false>(acc, o1, o0, toff, 0, 0, 0, dv, rsV, 0);
    }
    {
      const float sc[5] = {QSCALE, QSCALE, QSCALE, QSCALE, QSCALE};
      write_img(w == 3 ? L_P1 : L_K1 + w * 10240, acc, sc, lane);
    }
    BARRIER();
    STAMP(2);
    dma_unit(vrb, dv, rsV, TILE(1) * 32768 + w * 128);
    dma_unit(vrb + 4096, dv, rsV, TILE(1) * 32768 + 16384 + w * 128);
    zero_acc(acc);
    BARRIER();
    bf16x8 va[2][4], vb[2][4];
#pragma unroll
    for (int hh = 0; hh < 2; ++hh)
#pragma unroll
      for (int dt = 0; dt < 4; ++dt) {
        const bf16x4 lo = ldstr(vra + toff[dt] + hh * 4096), hi = ldstr(vra + toff[dt] + hh * 4096 + 512);
        va[hh][dt] = __builtin_shufflevector(lo, hi, 0, 1, 2, 3, 4, 5, 6, 7);
      }
    unsigned ta[4];
#pragma unroll
    for (int dt = 0; dt < 4; ++dt) ta[dt] = vra + toff[dt];
    unsigned pa = L_P0 + boff;
    asm volatile("" : "+v"(pa));
    rsrc_t rsD = rsV;
    unsigned dstep = 16384;
    auto pv_tile = [&](int t, auto par_c, bf16x8 (&vc)[2][4], bf16x8 (&vn)[2][4]) __attribute__((always_inline)) {
      constexpr int PAR = decltype(par_c)::value;
      unsigned so;
      if (t < 14) so = TILE(t + 2) * 32768 + w * 128;
      else so = ((rotC + t - 14) & 3) * 64 * WOROW + wid * 128;
      const unsigned dsl = vra + PAR * 8192;
      bf16x8 pf[5][2];
#define RDP(rt) { pf[rt][0] = ldsplit(pa + PAR * 10240 + (rt) * 1024); pf[rt][1] = ldsplit(pa + PAR * 10240 + 5120 + (rt) * 1024); }
#define PM(i, rt) if (!(ABL & 8)) mfma_acc(acc[(i) & 3][rt], vc[(i) >> 2][(i) & 3], pf[rt][(i) >> 2]);
#define RDV(dt, hh)                                                                                                  \
  vn[hh][dt] = __builtin_shufflevector(ldstr(ta[dt] + (PAR ^ 1) * 8192 + (hh) * 4096), ldstr(ta[dt] + (PAR ^ 1) * 8192 + (hh) * 4096 + 512), 0, 1, 2, 3, 4, 5, 6, 7);
#define BURST(rt)                                                                                                  \
  SBAR(); TR(2 * rt + 1); PM(0, rt) SBAR(); PRIO_HI(); SBAR(); PM(1, rt) PM(2, rt) PM(3, rt) PM(4, rt) PM(5, rt) PM(6, rt) PM(7, rt) SBAR(); PRIO_LO(); TR(2 * rt + 2); SBAR();
#define PD(P)                                                                                                      \
  if (!(ABL & 2)) {                                                                                                \
    if (((P) & 3) == 0) dma0<0>(dsl + ((P) >> 2) * 4096, dv[0], rsD, (P) ? so + dstep : so);                       \
    else dmaN<((P) & 3) * 1024>(dv[(P) & 3], rsD, (P) >> 2 ? so + dstep : so);                                     \
  }
      TR(11);
      BARRIER();
      TR(12);
      TRSAVE(t == 7);
      TR(0);
      unsigned fl = *(LAS unsigned*)(L_FLAG + (t + 1) * 4);
      RDP(0) RDP(1)
      SBAR();
      PD(0)
      SBAR();
      fl = __builtin_amdgcn_readfirstlane(fl);
      if (__builtin_expect(fl != 0, 0)) {
        if (t > 0) {
#pragma unroll
          for (int rt = 0; rt < 5; ++rt) {
            const float f = ex2(-read_D(rt, fl, row));
#pragma unroll
            for (int dt = 0; dt < 4; ++dt) acc[dt][rt] *= f;
          }
        }
        BARRIER();
        RDP(0) RDP(1)
      }
      BURST(0)
      RDP(2) SBAR();
      PD(1) PD(2)
      BURST(1)
      RDP(3) SBAR();
      WAITVM(7);
      RDV(0, 0) RDV(1, 0) SBAR();
      PD(3)
      BURST(2)
      RDP(4) RDV(2, 0) RDV(3, 0) SBAR();
      PD(4) PD(5)
      BURST(3)
      TR(13);
      WAITVM(6);
      RDV(0, 1) RDV(1, 1) RDV(2, 1) RDV(3, 1) SBAR();
      PD(6) PD(7)
      if (PAR == 1 && t == 15) BARRIER();
      BURST(4)
#undef PD
#undef BURST
#undef RDV
#undef PM
#undef RDP
    };
#pragma unroll 1
    for (int t = 0; t < 16; t += 2) {
      if (t == 14) { unit_voff(dv, lane_now(), WOROW); rsD = rsWO; dstep = 32 * WOROW; }
      pv_tile(t, std::integral_constant<int, 0>{}, va, vb);
      pv_tile(t + 1, std::integral_constant<int, 1>{}, vb, va);
      if (t == 10) STAMP(4);
    }
    MFMA_DRAIN();
    STAMP(5);
    float inv[5];
#pragma unroll
    for (int rt = 0; rt < 5; ++rt) {
      float l = 0.f;
#pragma unroll
      for (int ww = 0; ww < 4; ++ww) l += ldsf(ls_addr(ww, rt, row));
      inv[rt] = 1.f / l;
    }
    if (w == 3) WAITVM(0);
    write_img(w == 0 ? L_P0 : w == 1 ? L_P1 : w == 2 ? L_MS : L_OI3, acc, inv, lane);
    hb0 = vra; hb1 = vrb;
  }

  BARRIER();
  STAMP(6);
  if (wid < 7) {
    const int lane = lane_now();
    const unsigned boff = lane * 8, boff1 = boff + 5120;
    unsigned toff[4], dv[4];
#pragma unroll
    for (int dt = 0; dt < 4; ++dt) toff[dt] = off_tr(lane, dt);
    unit_voff(dv, lane, WOROW);
    f32x4 acc[4][5];
    zero_acc(acc);
    OPS o0, o1;
    auto imgof = [&](unsigned cc) __attribute__((always_inline)) { cc &= 3; return cc == 0 ? L_P0 : cc == 1 ? L_P1 : cc == 2 ? L_MS : L_OI3; };
    WAITVM(12);
    step<false, true, false>(acc, o1, o0, toff, hb0, imgof(rotC) + boff, 0, dv, rsWO, 0);
#pragma unroll 1
    for (int s = 0; s < 2; ++s) {
      const unsigned so = ((rotC + s + 2) & 3) * 64 * WOROW + wid * 128;
      WAITVM(8);
      step<true, true, true>(acc, o0, o1, toff, hb0 + 4096, imgof(rotC + s) + boff1, hb0, dv, rsWO, so);
      WAITVM(8);
      step<true, true, true>(acc, o1, o0, toff, hb1, imgof(rotC + s + 1) + boff, hb0 + 4096, dv, rsWO, so + 32 * WOROW);
      const unsigned t0 = hb0; hb0 = hb1; hb1 = t0;
    }
    WAITVM(8);
    step<true, true, false>(acc, o0, o1, toff, hb0 + 4096, imgof(rotC + 2) + boff1, 0, dv, rsWO, 0);
    WAITVM(4);
    step<true, true, false>(acc, o1, o0, toff, hb1, imgof(rotC + 3) + boff, 0, dv, rsWO, 0);
    WAITVM(0);
    step<true, true, false>(acc, o0, o1, toff, hb1 + 4096, imgof(rotC + 3) + boff1, 0, dv, rsWO, 0);
    step<true, false, false>(acc, o1, o0, toff, 0, 0, 0, dv, rsWO, 0);
    STAMP(7);
    char* slab = ws + (size_t)((b * 2 + ph) * 16 + h) * SLAB + (size_t)wid * 10240 + lane * 16;
#pragma unroll
    for (int rt = 0; rt < 5; ++rt)
#pragma unroll
      for (int np = 0; np < 2; ++np) {
        const f32x4 u = acc[2 * np][rt], v = acc[2 * np + 1][rt];
        *(u32x4*)(slab + (rt * 2 + np) * 1024) = u32x4{cvtpk(u[0], u[1]), cvtpk(u[2], u[3]), cvtpk(v[0], v[1]), cvtpk(v[2], v[3])};
      }
  }
}

__global__ __launch_bounds__(256) void gqa_reduce(const char* __restrict__ ws, char* __restrict__ out) {
  const int id = blockIdx.x * 256 + threadIdx.x;
  const int grp = id / 4480, ci = id % 4480;
  const char* src = ws + (size_t)grp * 16 * SLAB + ci * 16;
  u32x4 v[16];
#pragma unroll
  for (int hh = 0; hh < 16; ++hh) v[hh] = *(const u32x4*)(src + (size_t)hh * SLAB);
  float s[8] = {0.f, 0.f, 0.f, 0.f, 0.f, 0.f, 0.f, 0.f};
#pragma unroll
  for (int hh = 0; hh < 16; ++hh)
#pragma unroll
    for (int i = 0; i < 4; ++i) { s[2 * i] += __uint_as_float(v[hh][i] << 16); s[2 * i + 1] += __uint_as_float(v[hh][i] & 0xffff0000u); }
  const int piece = ci >> 6, lane = ci & 63, sl = piece / 10, rt = (piece % 10) >> 1, np = piece & 1;
  const int row = 16 * rt + (lane & 15), n0 = 64 * sl + 32 * np + 4 * (lane >> 4);
  char* dst = out + ((size_t)(grp * 80 + row) * DM + n0) * 2;
  *(u32x2*)dst = u32x2{cvtpk(s[0], s[1]), cvtpk(s[2], s[3])};
  *(u32x2*)(dst + 32) = u32x2{cvtpk(s[4], s[5]), cvtpk(s[6], s[7])};
}

extern "C" void kernel_launch(void* const* d_in, const int* in_sizes, int n_in, void* d_out, int out_size, void* d_ws, size_t ws_size,
                              hipStream_t stream) {
  (void)in_sizes; (void)n_in; (void)out_size; (void)ws_size;
  (void)hipFuncSetAttribute((const void*)gqa_fused, hipFuncAttributeMaxDynamicSharedMemorySize, L_TOTAL);
  gqa_fused<<<256, 512, L_TOTAL, stream>>>(d_in[0], d_in[1], d_in[2], d_in[3], d_in[4], (char*)d_ws);
  gqa_reduce<<<280, 256, 0, stream>>>((const char*)d_ws, (char*)d_out);
}
```

```cpp
#include <hip/hip_runtime.h>
#include <hip/hip_bf16.h>
#include <type_traits>
#define PP 1
#define QD 1
#define PRQ 2
#define PRP 0
#define ABL 0
#pragma clang diagnostic ignored "-Wint-to-pointer-cast"

typedef __bf16 bf16x8 __attribute__((ext_vector_type(8)));
typedef __bf16 bf16x4 __attribute__((ext_vector_type(4)));
typedef __bf16 bf16x2 __attribute__((ext_vector_type(2)));
typedef float f32x4 __attribute__((ext_vector_type(4)));
typedef float f32x2 __attribute__((ext_vector_type(2)));
typedef unsigned u32x4 __attribute__((ext_vector_type(4)));
typedef unsigned u32x2 __attribute__((ext_vector_type(2)));
#define LAS __attribute__((address_space(3)))
#define DI __device__ __forceinline__
typedef __amdgpu_buffer_rsrc_t rsrc_t;

constexpr int NB = 8, SQ = 160, DM = 448, HQ = 16, HKV = 8, SK = 1024, DH = 256;
constexpr unsigned XROW = DM * 2, WQROW = HQ * DH * 2, KVROW = DH * 2, WOROW = DM * 2;
constexpr unsigned SLAB = 80 * DM * 2;
constexpr unsigned WS_FLAG = 0, WS_EPOCH = 32768, WS_SLAB = 65536;
typedef __attribute__((address_space(1))) unsigned long long gu64;
constexpr unsigned KSLOT = 8448;
constexpr unsigned L_K0 = 0, L_K1 = 4 * KSLOT, L_VR = 8 * KSLOT, L_P0 = L_VR + 65536, L_P1 = L_P0 + 10240, L_MS = L_P1 + 10240, L_TOTAL = L_MS + 10240;
static_assert(L_TOTAL == 163840, "LDS");
constexpr unsigned L_FLAG = L_MS;
constexpr unsigned L_DLT = L_MS + 128;
constexpr unsigned L_OI3 = L_VR + 3 * 16384;
constexpr float QSCALE = 0.0625f * 1.4426950408889634f;
#define PTHR 65536.f
#define PLOW (1.f / 65536.f)

#define STAMP(k)
#define ABL_KSZ(x) ((ABL & 1) ? 0 : (x))
#define ABL_VSZ(x) ((ABL & 1024) ? 0 : (x))
#define SBAR() __builtin_amdgcn_sched_barrier(0)
#define TR(k)
#define TRSAVE(cond)
#define PRIO_HI() __builtin_amdgcn_s_setprio(1)
#define PRIO_LO() __builtin_amdgcn_s_setprio(0)
#define WAITVM(n) asm volatile("s_waitcnt vmcnt(%0)" ::"i"(n) : "memory")
#define WAITLGKM0() do { asm volatile("" ::: "memory"); __builtin_amdgcn_s_waitcnt(0xC07F); asm volatile("" ::: "memory"); } while (0)
#define BARRIER() do { WAITLGKM0(); asm volatile("s_barrier" ::: "memory"); } while (0)

DI void dma(unsigned ldsaddr, unsigned voff, rsrc_t rs, unsigned soff) {
  asm volatile("s_mov_b32 m0, %0\n\ts_nop 0\n\tbuffer_load_dwordx4 %1, %2, %3 offen lds" ::"s"(ldsaddr), "v"(voff), "s"(rs), "s"(soff) : "memory");
}
DI int lane_now() { unsigned z = 0; asm volatile("" : "+v"(z)); return __builtin_amdgcn_mbcnt_hi(~0u, __builtin_amdgcn_mbcnt_lo(~0u, z)); }
DI bf16x8 lds128(unsigned a) { return *(LAS bf16x8*)a; }
DI bf16x4 ldstr(unsigned a) { return __builtin_amdgcn_ds_read_tr16_b64_v4bf16((LAS bf16x4*)a); }
DI float ldsf(unsigned a) { return *(LAS float*)a; }
DI unsigned cvtpk(float lo, float hi) { f32x2 v = {lo, hi}; return __builtin_bit_cast(unsigned, __builtin_convertvector(v, bf16x2)); }
DI f32x4 mfma(bf16x8 a, bf16x8 b, f32x4 c) { return __builtin_amdgcn_mfma_f32_16x16x32_bf16(a, b, c, 0, 0, 0); }
DI void mfma_acc(f32x4& c, bf16x8 a, bf16x8 b) { asm volatile("v_mfma_f32_16x16x32_bf16 %0, %1, %2, %0" : "+v"(c) : "v"(a), "v"(b)); }
DI void mfma_init(f32x4& d, bf16x8 a, bf16x8 b, f32x4 c) { asm volatile("v_mfma_f32_16x16x32_bf16 %0, %1, %2, %3" : "=&v"(d) : "v"(a), "v"(b), "v"(c)); }
#define MFMA_DRAIN() asm volatile("s_nop 7\n\ts_nop 1" ::: "memory")
DI float ex2(float x) { return __builtin_amdgcn_exp2f(x); }
DI unsigned off_cE(int lane) { return (unsigned)((lane & 7) ^ (((lane >> 4) & 1) << 1)) << 4; }
DI unsigned off_b(int lane) { return (lane & 15) * 128 + (((lane >> 4) ^ (((lane & 15) >> 1) & 7)) << 4); }
DI unsigned off_tr(int lane, int dt) {
  const int g = lane >> 4, qq = (lane >> 2) & 3, pp = lane & 3, f = ((qq >> 1) & 1) | ((g & 1) << 1);
  return (8 * g + qq) * 128 + ((dt ^ f) << 5) + pp * 8;
}
DI unsigned off_w(int lane, int blk) {
  const int row = lane & 15, g = lane >> 4;
  return (blk >> 1) * 5120 + (g & 1) * 512 + ((2 * (blk & 1) + (g >> 1)) * 16 + row) * 8;
}
DI bf16x8 ldsplit(unsigned a) {
  const bf16x4 lo = *(LAS bf16x4*)a, hi = *(LAS bf16x4*)(a + 512);
  return __builtin_shufflevector(lo, hi, 0, 1, 2, 3, 4, 5, 6, 7);
}
DI float xmax4(float m) { m = fmaxf(m, __shfl_xor(m, 16)); return fmaxf(m, __shfl_xor(m, 32)); }

template <int IMM> DI void dma0(unsigned ldsaddr, unsigned voff, rsrc_t rs, unsigned soff) {
  asm volatile("s_mov_b32 m0, %0\n\ts_nop 0\n\tbuffer_load_dwordx4 %1, %2, %3 offen offset:%4 lds" ::"s"(ldsaddr), "v"(voff), "s"(rs), "s"(soff), "i"(IMM) : "memory");
}
template <int IMM> DI void dmaN(unsigned voff, rsrc_t rs, unsigned soff) {
  asm volatile("buffer_load_dwordx4 %0, %1, %2 offen offset:%3 lds" ::"v"(voff), "s"(rs), "s"(soff), "i"(IMM) : "memory");
}
DI void unit_voff(unsigned (&v)[4], int lane, unsigned stride) {
  const unsigned e = (lane >> 3) * stride + off_cE(lane);
#pragma unroll
  for (int p = 0; p < 4; ++p) v[p] = ((p & 1) ? e ^ 64 : e) + p * (8 * stride - 1024);
}
DI void dma_unit(unsigned ldsaddr, const unsigned (&v)[4], rsrc_t rs, unsigned soff) {
  dma0<0>(ldsaddr, v[0], rs, soff); dmaN<1024>(v[1], rs, soff); dmaN<2048>(v[2], rs, soff); dmaN<3072>(v[3], rs, soff);
}
struct OPS { bf16x8 a[4], b[5]; };
template <bool MMA, bool READ, bool DMA, bool SPLIT = true>
DI void step(f32x4 (&acc)[4][5], const OPS& cur, OPS& nxt, const unsigned (&toff)[4], unsigned aslot, unsigned baddr,
             unsigned dslot, const unsigned (&dv)[4], rsrc_t rs, unsigned soff) {
  bf16x4 lo, hi;
  unsigned aa;
#pragma unroll
  for (int i = 0; i < 20; ++i) {
    if (MMA && !((ABL & 8) && DMA)) acc[i & 3][i >> 2] = mfma(cur.a[i & 3], cur.b[i >> 2], acc[i & 3][i >> 2]);
    if (READ && !((ABL & 4) && DMA)) {
      if (i == 0) nxt.b[0] = SPLIT ? ldsplit(baddr) : lds128(baddr);
      if (i >= 1 && i <= 8) {
        const int dt = (i - 1) >> 1;
        if ((i - 1) & 1) { hi = ldstr(aa + 512); nxt.a[dt] = __builtin_shufflevector(lo, hi, 0, 1, 2, 3, 4, 5, 6, 7); }
        else { aa = aslot + toff[dt]; lo = ldstr(aa); }
      }
      if (i >= 9 && i <= 12) nxt.b[i - 8] = SPLIT ? ldsplit(baddr + (i - 8) * 1024) : lds128(baddr + (i - 8) * 2048);
    }
    if (DMA && !(ABL & 2)) {
      if (i == 5) dma0<0>(dslot, dv[0], rs, soff);
      if (i == 9) dmaN<1024>(dv[1], rs, soff);
      if (i == 13) dmaN<2048>(dv[2], rs, soff);
      if (i == 17) dmaN<3072>(dv[3], rs, soff);
    }
    SBAR();
  }
}
DI void read_b(OPS& o, unsigned addr) {
#pragma unroll
  for (int rt = 0; rt < 5; ++rt) o.b[rt] = ldsplit(addr + rt * 1024);
}
DI void zero_acc(f32x4 (&acc)[4][5]) {
#pragma unroll
  for (int dt = 0; dt < 4; ++dt)
#pragma unroll
    for (int rt = 0; rt < 5; ++rt) acc[dt][rt] = f32x4{0.f, 0.f, 0.f, 0.f};
}
DI void write_img(unsigned img, const f32x4 (&acc)[4][5], const float (&sc)[5], int lane) {
#pragma unroll
  for (int dt = 0; dt < 4; ++dt) {
    const unsigned a = img + off_w(lane, dt);
#pragma unroll
    for (int rt = 0; rt < 5; ++rt) {
      u32x2 w = {cvtpk(acc[dt][rt][0] * sc[rt], acc[dt][rt][1] * sc[rt]), cvtpk(acc[dt][rt][2] * sc[rt], acc[dt][rt][3] * sc[rt])};
      *(LAS u32x2*)(a + rt * 1024) = w;
    }
  }
}
DI float read_D(int rt, unsigned fl, int row) {
  float d[4];
#pragma unroll
  for (int w = 0; w < 4; ++w) {
    const float v = ldsf(L_DLT + (w * 80 + rt * 16 + row) * 4);
    d[w] = ((fl >> (8 * w + rt)) & 1) ? v : 0.f;
  }
  return fmaxf(fmaxf(d[0], d[1]), fmaxf(d[2], d[3]));
}
DI float fadd_s(float a, float b) { float r = a + b; asm("" : "+v"(r)); return r; }
DI unsigned ls_addr(int w, int rt, int row) { return (rt < 4 ? L_K0 + rt * 64 : L_K1) + w * KSLOT + 8192 + row * 4; }

constexpr unsigned long long mkord(const int (&o)[16]) { unsigned long long r = 0; for (int i = 0; i < 16; ++i) r |= (unsigned long long)o[i] << (4 * i); return r; }
constexpr int ORD0[16] = {0, 4, 1, 2, 3, 8, 5, 6, 7, 12, 9, 10, 11, 13, 14, 15};
constexpr int ORD1[16] = {1, 0, 5, 2, 3, 4, 9, 6, 7, 8, 13, 10, 11, 12, 14, 15};
constexpr int ORD2[16] = {2, 0, 1, 6, 3, 4, 5, 10, 7, 8, 9, 14, 11, 12, 13, 15};
constexpr int ORD3[16] = {3, 0, 1, 2, 7, 4, 5, 6, 11, 8, 9, 10, 15, 12, 13, 14};

__global__ __launch_bounds__(512, 2) void gqa_fused(const void* __restrict__ x, const void* __restrict__ kc, const void* __restrict__ vc,
                                                    const void* __restrict__ wq, const void* __restrict__ wo, char* ws, char* __restrict__ out) {
  STAMP(0);
  const int wid = __builtin_amdgcn_readfirstlane(threadIdx.x >> 6);
  const int bid = blockIdx.x, kvh = bid & 7, q = (bid >> 3) & 3, b = bid >> 5, hg = q >> 1, ph = q & 1, h = 2 * kvh + hg;
  const unsigned rotA = (unsigned)(2 * b + ph) % 7u, rotC = (unsigned)(2 * b + ph) & 3u;
  const unsigned long long ord = q == 0 ? mkord(ORD0) : q == 1 ? mkord(ORD1) : q == 2 ? mkord(ORD2) : mkord(ORD3);
#define TILE(t) ((ABL & 4096) ? (unsigned)(t) : ((unsigned)(ord >> (4 * (t))) & 15u))
  const rsrc_t rsWO = __builtin_amdgcn_make_buffer_rsrc((char*)wo + (size_t)h * DH * WOROW, 0, wid == 7 ? 0 : DH * WOROW, 0x00020000);
  const rsrc_t rsV = __builtin_amdgcn_make_buffer_rsrc((char*)vc + (size_t)(((ABL & 8192) ? 0 : b) * HKV + kvh) * SK * KVROW, 0, ABL_VSZ(SK * KVROW), 0x00020000);
  const int w = wid & 3;
  const unsigned ks0 = L_K0 + w * KSLOT, ks1 = L_K1 + w * KSLOT, vra = L_VR + w * 16384, vrb = vra + 8192;
  unsigned hb0, hb1;
  unsigned long long epoch = 0;

  if (wid < 4) {
    const rsrc_t rsK = __builtin_amdgcn_make_buffer_rsrc((char*)kc + (size_t)(((ABL & 8192) ? 0 : b) * HKV + kvh) * SK * KVROW - 128, 0, ABL_KSZ(SK * KVROW + 128), 0x00020000);
    unsigned kv0, kra, pwa;
    {
      const int lane = lane_now(), row = lane & 15, g = lane >> 4;
      kv0 = (lane >> 5) * 512 + (((lane & 31) ^ (lane >> 5)) << 4);
      kra = ks0 + (row >> 1) * 1056 + (row & 1) * 512 + ((g ^ (row & 1)) << 4);
      pwa = L_P0 + off_w(lane, w);
    }
#define DMAK(P, slot, so)                                                                     \
  do {                                                                                        \
    if (((P) & 3) == 0) dma0<0>((slot) + ((P) >> 2) * 4224, kv0, rsK, (so) + ((P) >> 2) * 4096); \
    else dmaN<((P) & 3) * 1056>(kv0, rsK, (so) + ((P) >> 2) * 4096 - ((P) & 3) * 32);           \
  } while (0)
    auto dmaK = [&](unsigned slot, unsigned tile) __attribute__((always_inline)) {
      const unsigned so = tile * 32768 + w * 8192 + 128;
      DMAK(0, slot, so); DMAK(1, slot, so); DMAK(2, slot, so); DMAK(3, slot, so);
      DMAK(4, slot, so); DMAK(5, slot, so); DMAK(6, slot, so); DMAK(7, slot, so);
    };
    bf16x8 kf[8];
    {
      const rsrc_t rsX = __builtin_amdgcn_make_buffer_rsrc((char*)x + (size_t)(b * SQ + ph * 80) * XROW, 0, 80 * XROW, 0x00020000);
      const rsrc_t rsWQ = __builtin_amdgcn_make_buffer_rsrc((char*)wq, 0, DM * WQROW, 0x00020000);
      const int lane = lane_now(), r8 = lane >> 3, ch = lane & 7;
      unsigned wv[4], xv[3];
      unit_voff(wv, lane, WQROW);
#pragma unroll
      for (int i = 0; i < 3; ++i) { const int o = i == 2 ? 12 : 8 * i, r = 20 * w + o + r8; xv[i] = r * XROW + ((ch ^ ((r >> 1) & 7)) << 4) - o * 128; }
      unsigned cc = rotA, as0 = vrb, as1 = vra, as2 = ks1, xs0 = L_P0, xs1 = L_P1, xs2 = L_MS;
      auto issue = [&]() __attribute__((always_inline)) {
        dma0<0>(xs0 + 20 * w * 128, xv[0], rsX, cc * 128); dmaN<1024>(xv[1], rsX, cc * 128); dmaN<1536>(xv[2], rsX, cc * 128);
        const unsigned so = cc * 64 * WQROW + h * 512 + w * 128;
        dma_unit(as0, wv, rsWQ, so);
        dma_unit(as0 + 4096, wv, rsWQ, so + 32 * WQROW);
        cc = cc == 6 ? 0 : cc + 1;
        unsigned t0 = as0; as0 = as1; as1 = as2; as2 = t0;
        t0 = xs0; xs0 = xs1; xs1 = xs2; xs2 = t0;
      };
      issue();
#pragma unroll 1
      for (int s = 0; s < 6; ++s) {
        issue();
        WAITVM(11);
        BARRIER();
        if (s == 0) STAMP(1);
      }
      dmaK(ks0, TILE(0));
      unit_voff(wv, lane, KVROW);
      dma_unit(vra, wv, rsV, TILE(0) * 32768 + w * 128);
      dma_unit(vra + 4096, wv, rsV, TILE(0) * 32768 + 16384 + w * 128);
      WAITVM(16);
      BARRIER();
    }
    WAITVM(8);
#pragma unroll
    for (int ks = 0; ks < 8; ++ks) kf[ks] = lds128(kra + ks * 64);
    if (w == 0) { const int lane = lane_now(); if (lane < 32) *(LAS unsigned*)(L_FLAG + lane * 4) = 0u; }
    WAITLGKM0();
    dmaK(ks0, TILE(1));
    BARRIER();
    STAMP(2);
    bf16x8 qf[5][8];
    {
      const unsigned l8 = lane_now() * 8;
#pragma unroll
      for (int rt = 0; rt < 5; ++rt)
#pragma unroll
        for (int ks = 0; ks < 8; ++ks) {
          const unsigned img = (ks >> 1) == 3 ? L_P1 : L_K1 + (ks >> 1) * 10240;
          qf[rt][ks] = ldsplit(img + (ks & 1) * 5120 + rt * 1024 + l8);
        }
    }
    f32x4 negm[5];
    float lsum[5];
#pragma unroll
    for (int rt = 0; rt < 5; ++rt) { negm[rt] = f32x4{0.f, 0.f, 0.f, 0.f}; lsum[rt] = 0.f; }
    f32x4 S0 = negm[0];
#pragma unroll
    for (int ks = 0; ks < 8; ++ks) S0 = mfma(kf[ks], qf[0][ks], S0);
    WAITVM(8);
    BARRIER();
    auto fixup = [&](unsigned fl, unsigned pprev, f32x4& Sa, f32x4& Sb) __attribute__((always_inline)) {
      const int row = lane_now() & 15;
#pragma unroll
      for (int rt = 0; rt < 5; ++rt) {
        const float D = read_D(rt, fl, row);
        const float own = ((fl >> (8 * w + rt)) & 1) ? ldsf(L_DLT + (w * 80 + rt * 16 + row) * 4) : 0.f;
        const float f = ex2(own - D);
        u32x2 pk = *(LAS u32x2*)(pprev + rt * 1024);
        const float a0 = __uint_as_float(pk[0] << 16) * f, a1 = __uint_as_float(pk[0] & 0xffff0000u) * f;
        const float a2 = __uint_as_float(pk[1] << 16) * f, a3 = __uint_as_float(pk[1] & 0xffff0000u) * f;
        *(LAS u32x2*)(pprev + rt * 1024) = u32x2{cvtpk(a0, a1), cvtpk(a2, a3)};
        lsum[rt] *= f;
        negm[rt] -= D;
        if (rt == 0) Sa -= D;
        if (rt == 1) Sb -= D;
        SBAR();
      }
      BARRIER();
    };
    unsigned dslot = ks1;
    const unsigned pwsum = 2 * pwa + 10240, krsum = 2 * kra + L_K1;
    auto qk_tile = [&](int t, auto wo_c) __attribute__((always_inline)) {
      constexpr bool WO = decltype(wo_c)::value;
      unsigned so, wv[4];
      if constexpr (WO) { unit_voff(wv, lane_now(), WOROW); so = ((rotC + t - 14) & 3) * 64 * WOROW + w * 128; }
      else so = TILE(t + 2) * 32768 + w * 8192 + 128;
      unsigned fl = *(LAS unsigned*)(L_FLAG + t * 4);
      f32x4 S1;
      float p0, p1, p2, p3, ts, tm;
      u32x2 pk;
#define QDMA(P)                                                                                                   \
  do {                                                                                                            \
    if constexpr (WO) {                                                                                           \
      if (((P) & 3) == 0) dma0<0>(dslot + ((P) >> 2) * 4096, wv[0], rsWO, so + ((P) >> 2) * 32 * WOROW);           \
      else dmaN<((P) & 3) * 1024>(wv[(P) & 3], rsWO, so + ((P) >> 2) * 32 * WOROW);                                \
    } else if (!(ABL & 2048)) DMAK(P, dslot, so);                                                                                    \
  } while (0)
#define QM(i, RN, SN)                                                                                              \
  if (ABL & 16) { SN = negm[RN]; asm volatile("" : "+v"(SN) : "v"(kf[i]), "v"(qf[RN][i])); }                       \
  else if ((i) == 0) mfma_init(SN, kf[i], qf[RN][i], negm[RN]); else mfma_acc(SN, kf[i], qf[RN][i]);
#define QKBLOCK(RC, SC, RN, SN)                                                                                    \
  TR(2 * RC); QM(0, RN, SN) SBAR(); PRIO_HI(); p0 = ex2(SC[0]); SBAR();                                                        \
  QM(1, RN, SN) SBAR(); p1 = ex2(SC[1]); SBAR();                                                                   \
  QM(2, RN, SN) SBAR(); p2 = ex2(SC[2]); SBAR();                                                                   \
  QM(3, RN, SN) SBAR(); p3 = ex2(SC[3]); SBAR();                                                                   \
  QM(4, RN, SN) SBAR(); ts = fadd_s(p0, p1); pk[0] = cvtpk(p0, p1); SBAR();                                        \
  QM(5, RN, SN) SBAR(); p0 = fadd_s(p2, p3); pk[1] = cvtpk(p2, p3); SBAR();                                        \
  QM(6, RN, SN) SBAR(); ts = fadd_s(ts, p0); if (RC == 0) fl = __builtin_amdgcn_readfirstlane(fl); SBAR();         \
  QM(7, RN, SN) SBAR(); PRIO_LO(); TR(2 * RC + 1);                                                                                \
  if (RC == 0) {                                                                                                   \
    if (__builtin_expect(fl != 0, 0)) {                                                                            \
      MFMA_DRAIN();                                                                                                \
      fixup(fl, pwsum - pwa, SC, SN);                                                                              \
      p0 = ex2(SC[0]); p1 = ex2(SC[1]); p2 = ex2(SC[2]); p3 = ex2(SC[3]);                                          \
      ts = (p0 + p1) + (p2 + p3);                                                                                  \
      pk = u32x2{cvtpk(p0, p1), cvtpk(p2, p3)};                                                                    \
    }                                                                                                              \
    SBAR();                                                                                                        \
  }                                                                                                                \
  if (RC == 3) {                                                                                                   \
    TR(13); WAITVM(6); TR(14);                                                                                     \
    _Pragma("unroll") for (int ks = 0; ks < 8; ++ks) kf[ks] = lds128(kra + ks * 64);                               \
    SBAR();                                                                                                        \
  }                                                                                                                \
  lsum[RC] = fadd_s(lsum[RC], ts); tm = RC == 0 ? ts : fadd_s(tm, ts);                                             \
  *(LAS u32x2*)(pwa + RC * 1024) = pk; SBAR();
      SBAR();
      QKBLOCK(0, S0, 1, S1)
      QDMA(0); QDMA(1); SBAR();
      QKBLOCK(1, S1, 2, S0)
      QDMA(2); QDMA(3); SBAR();
      QKBLOCK(2, S0, 3, S1)
      QDMA(4); QDMA(5); SBAR();
      QKBLOCK(3, S1, 4, S0)
      QKBLOCK(4, S0, 0, S1)
      QDMA(6); QDMA(7); SBAR();
      S0 = S1;
#undef QKBLOCK
#undef QM
#undef QDMA
      bool bad = !(tm <= PTHR);
      if (t == 0) bad |= !(fminf(fminf(fminf(lsum[0], lsum[1]), fminf(lsum[2], lsum[3])), lsum[4]) >= PLOW);
      if (!(ABL & 128)) if (__builtin_expect(__any(bad), 0)) {
        const int lane = lane_now();
#pragma unroll
        for (int ks = 0; ks < 8; ++ks)
          kf[ks] = __builtin_bit_cast(bf16x8, __builtin_amdgcn_raw_buffer_load_b128(rsK, (lane & 15) * 512 + (lane >> 4) * 16 + ks * 64, TILE(t) * 32768 + w * 8192 + 128, 0));
#pragma unroll
        for (int rt = 0; rt < 5; ++rt) {
          f32x4 S = negm[rt];
#pragma unroll
          for (int ks = 0; ks < 8; ++ks) S = mfma(kf[ks], qf[rt][ks], S);
          float dl = ceilf(xmax4(fmaxf(fmaxf(S[0], S[1]), fmaxf(S[2], S[3]))));
          if (t > 0) dl = fmaxf(dl, 0.f);
          p0 = ex2(S[0] - dl); p1 = ex2(S[1] - dl); p2 = ex2(S[2] - dl); p3 = ex2(S[3] - dl);
          lsum[rt] = (t > 0 && lsum[rt] <= 1.0e18f) ? lsum[rt] * ex2(-dl) : (p0 + p1) + (p2 + p3);
          *(LAS u32x2*)(pwa + rt * 1024) = u32x2{cvtpk(p0, p1), cvtpk(p2, p3)};
          if (lane < 16) *(LAS float*)(L_DLT + (w * 80 + rt * 16 + lane) * 4) = dl;
          SBAR();
        }
#pragma unroll
        for (int ks = 0; ks < 8; ++ks) kf[ks] = lds128(kra + ks * 64);
        *(LAS unsigned char*)(L_FLAG + (t + 1) * 4 + w) = 31;
      }
      kra = krsum - kra;
      pwa = pwsum - pwa;
      dslot = ks0 + ks1 - dslot;
      TR(10);
      WAITLGKM0();
      TR(11);
      BARRIER();
      TR(12);
      TRSAVE(t == 7);
    };
#pragma unroll 1
    for (int t = 0; t < 14; ++t) {
      qk_tile(t, std::false_type{});
    }
#pragma unroll 1
    for (int t = 14; t < 16; ++t) qk_tile(t, std::true_type{});
    {
      const unsigned fl = __builtin_amdgcn_readfirstlane(*(LAS unsigned*)(L_FLAG + 64));
      f32x4 dummy = {0.f, 0.f, 0.f, 0.f};
      if (__builtin_expect(fl != 0, 0)) fixup(fl, pwsum - pwa, dummy, dummy);
    }
    {
      const int lane = lane_now();
#pragma unroll
      for (int rt = 0; rt < 5; ++rt) {
        float l = lsum[rt];
        l += __shfl_xor(l, 16);
        l += __shfl_xor(l, 32);
        if (lane < 16) *(LAS float*)ls_addr(w, rt, lane) = l;
      }
    }
    BARRIER();
    STAMP(5);
    hb0 = ks1; hb1 = ks0;
  } else {
    if (wid == 7) {
      const unsigned long long e = __hip_atomic_load((gu64*)(ws + WS_EPOCH + (b * 2 + ph) * 128), __ATOMIC_RELAXED, __HIP_MEMORY_SCOPE_AGENT);
      epoch = ((unsigned long long)__builtin_amdgcn_readfirstlane((unsigned)(e >> 32)) << 32) | __builtin_amdgcn_readfirstlane((unsigned)e);
    }
    const int lane = lane_now(), row = lane & 15;
    const unsigned xoff = off_b(lane), xoff1 = xoff ^ 64, boff = lane * 8, boff1 = boff + 5120;
    unsigned toff[4], dv[4];
#pragma unroll
    for (int dt = 0; dt < 4; ++dt) toff[dt] = off_tr(lane, dt);
    unit_voff(dv, lane, KVROW);
    f32x4 acc[4][5];
    zero_acc(acc);
    OPS o0, o1;
    {
      unsigned as0 = vrb, as1 = vra, as2 = ks1, xs0 = L_P0, xs1 = L_P1, xs2 = L_MS;
      BARRIER();
      STAMP(1);
      step<false, true, false, false>(acc, o1, o0, toff, as0, xs0 + xoff, 0, dv, rsV, 0);
#pragma unroll 1
      for (int s = 0; s < 6; ++s) {
        step<true, true, false, false>(acc, o0, o1, toff, as0 + 4096, xs0 + xoff1, 0, dv, rsV, 0);
        unsigned t0 = as0; as0 = as1; as1 = as2; as2 = t0;
        t0 = xs0; xs0 = xs1; xs1 = xs2; xs2 = t0;
        BARRIER();
        step<true, true, false, false>(acc, o1, o0, toff, as0, xs0 + xoff, 0, dv, rsV, 0);
      }
      step<true, true, false, false>(acc, o0, o1, toff, as0 + 4096, xs0 + xoff1, 0, dv, rsV, 0);
      step<true, false, false>(acc, o1, o0, toff, 0, 0, 0, dv, rsV, 0);
    }
    {
      const float sc[5] = {QSCALE, QSCALE, QSCALE, QSCALE, QSCALE};
      write_img(w == 3 ? L_P1 : L_K1 + w * 10240, acc, sc, lane);
    }
    BARRIER();
    STAMP(2);
    dma_unit(vrb, dv, rsV, TILE(1) * 32768 + w * 128);
    dma_unit(vrb + 4096, dv, rsV, TILE(1) * 32768 + 16384 + w * 128);
    zero_acc(acc);
    BARRIER();
    bf16x8 va[2][4], vb[2][4];
#pragma unroll
    for (int hh = 0; hh < 2; ++hh)
#pragma unroll
      for (int dt = 0; dt < 4; ++dt) {
        const bf16x4 lo = ldstr(vra + toff[dt] + hh * 4096), hi = ldstr(vra + toff[dt] + hh * 4096 + 512);
        va[hh][dt] = __builtin_shufflevector(lo, hi, 0, 1, 2, 3, 4, 5, 6, 7);
      }
    unsigned ta[4];
#pragma unroll
    for (int dt = 0; dt < 4; ++dt) ta[dt] = vra + toff[dt];
    unsigned pa = L_P0 + boff;
    asm volatile("" : "+v"(pa));
    rsrc_t rsD = rsV;
    unsigned dstep = 16384;
    auto pv_tile = [&](int t, auto par_c, bf16x8 (&vc)[2][4], bf16x8 (&vn)[2][4]) __attribute__((always_inline)) {
      constexpr int PAR = decltype(par_c)::value;
      unsigned so;
      if (t < 14) so = TILE(t + 2) * 32768 + w * 128;
      else so = ((rotC + t - 14) & 3) * 64 * WOROW + wid * 128;
      const unsigned dsl = vra + PAR * 8192;
      bf16x8 pf[5][2];
#define RDP(rt) { pf[rt][0] = ldsplit(pa + PAR * 10240 + (rt) * 1024); pf[rt][1] = ldsplit(pa + PAR * 10240 + 5120 + (rt) * 1024); }
#define PM(i, rt) if (!(ABL & 8)) mfma_acc(acc[(i) & 3][rt], vc[(i) >> 2][(i) & 3], pf[rt][(i) >> 2]);
#define RDV(dt, hh)                                                                                                  \
  vn[hh][dt] = __builtin_shufflevector(ldstr(ta[dt] + (PAR ^ 1) * 8192 + (hh) * 4096), ldstr(ta[dt] + (PAR ^ 1) * 8192 + (hh) * 4096 + 512), 0, 1, 2, 3, 4, 5, 6, 7);
#define BURST(rt)                                                                                                  \
  SBAR(); TR(2 * rt + 1); PM(0, rt) SBAR(); PRIO_HI(); SBAR(); PM(1, rt) PM(2, rt) PM(3, rt) PM(4, rt) PM(5, rt) PM(6, rt) PM(7, rt) SBAR(); PRIO_LO(); TR(2 * rt + 2); SBAR();
#define PD(P)                                                                                                      \
  if (!(ABL & 2)) {                                                                                                \
    if (((P) & 3) == 0) dma0<0>(dsl + ((P) >> 2) * 4096, dv[0], rsD, (P) ? so + dstep : so);                       \
    else dmaN<((P) & 3) * 1024>(dv[(P) & 3], rsD, (P) >> 2 ? so + dstep : so);                                     \
  }
      TR(11);
      BARRIER();
      TR(12);
      TRSAVE(t == 7);
      TR(0);
      unsigned fl = *(LAS unsigned*)(L_FLAG + (t + 1) * 4);
      RDP(0) RDP(1)
      SBAR();
      PD(0)
      SBAR();
      fl = __builtin_amdgcn_readfirstlane(fl);
      if (__builtin_expect(fl != 0, 0)) {
        if (t > 0) {
#pragma unroll
          for (int rt = 0; rt < 5; ++rt) {
            const float f = ex2(-read_D(rt, fl, row));
#pragma unroll
            for (int dt = 0; dt < 4; ++dt) acc[dt][rt] *= f;
          }
        }
        BARRIER();
        RDP(0) RDP(1)
      }
      BURST(0)
      RDP(2) SBAR();
      PD(1) PD(2)
      BURST(1)
      RDP(3) SBAR();
      WAITVM(7);
      RDV(0, 0) RDV(1, 0) SBAR();
      PD(3)
      BURST(2)
      RDP(4) RDV(2, 0) RDV(3, 0) SBAR();
      PD(4) PD(5)
      BURST(3)
      TR(13);
      WAITVM(6);
      RDV(0, 1) RDV(1, 1) RDV(2, 1) RDV(3, 1) SBAR();
      PD(6) PD(7)
      if (PAR == 1 && t == 15) BARRIER();
      BURST(4)
#undef PD
#undef BURST
#undef RDV
#undef PM
#undef RDP
    };
#pragma unroll 1
    for (int t = 0; t < 16; t += 2) {
      if (t == 14) { unit_voff(dv, lane_now(), WOROW); rsD = rsWO; dstep = 32 * WOROW; }
      pv_tile(t, std::integral_constant<int, 0>{}, va, vb);
      pv_tile(t + 1, std::integral_constant<int, 1>{}, vb, va);
      if (t == 10) STAMP(4);
    }
    MFMA_DRAIN();
    STAMP(5);
    float inv[5];
#pragma unroll
    for (int rt = 0; rt < 5; ++rt) {
      float l = 0.f;
#pragma unroll
      for (int ww = 0; ww < 4; ++ww) l += ldsf(ls_addr(ww, rt, row));
      inv[rt] = 1.f / l;
    }
    if (w == 3) WAITVM(0);
    write_img(w == 0 ? L_P0 : w == 1 ? L_P1 : w == 2 ? L_MS : L_OI3, acc, inv, lane);
    hb0 = vra; hb1 = vrb;
  }

  BARRIER();
  STAMP(6);
  if (wid < 7) {
    const int lane = lane_now();
    const unsigned boff = lane * 8, boff1 = boff + 5120;
    unsigned toff[4], dv[4];
#pragma unroll
    for (int dt = 0; dt < 4; ++dt) toff[dt] = off_tr(lane, dt);
    unit_voff(dv, lane, WOROW);
    f32x4 acc[4][5];
    zero_acc(acc);
    OPS o0, o1;
    auto imgof = [&](unsigned cc) __attribute__((always_inline)) { cc &= 3; return cc == 0 ? L_P0 : cc == 1 ? L_P1 : cc == 2 ? L_MS : L_OI3; };
    WAITVM(12);
    step<false, true, false>(acc, o1, o0, toff, hb0, imgof(rotC) + boff, 0, dv, rsWO, 0);
#pragma unroll 1
    for (int s = 0; s < 2; ++s) {
      const unsigned so = ((rotC + s + 2) & 3) * 64 * WOROW + wid * 128;
      WAITVM(8);
      step<true, true, true>(acc, o0, o1, toff, hb0 + 4096, imgof(rotC + s) + boff1, hb0, dv, rsWO, so);
      WAITVM(8);
      step<true, true, true>(acc, o1, o0, toff, hb1, imgof(rotC + s + 1) + boff, hb0 + 4096, dv, rsWO, so + 32 * WOROW);
      const unsigned t0 = hb0; hb0 = hb1; hb1 = t0;
    }
    WAITVM(8);
    step<true, true, false>(acc, o0, o1, toff, hb0 + 4096, imgof(rotC + 2) + boff1, 0, dv, rsWO, 0);
    WAITVM(4);
    step<true, true, false>(acc, o1, o0, toff, hb1, imgof(rotC + 3) + boff, 0, dv, rsWO, 0);
    WAITVM(0);
    step<true, true, false>(acc, o0, o1, toff, hb1 + 4096, imgof(rotC + 3) + boff1, 0, dv, rsWO, 0);
    step<true, false, false>(acc, o1, o0, toff, 0, 0, 0, dv, rsWO, 0);
    STAMP(7);
    const rsrc_t rsS = __builtin_amdgcn_make_buffer_rsrc(ws + WS_SLAB + (size_t)((b * 2 + ph) * 16 + h) * SLAB, 0, SLAB, 0x00020000);
#pragma unroll
    for (int rt = 0; rt < 5; ++rt)
#pragma unroll
      for (int np = 0; np < 2; ++np) {
        const f32x4 u = acc[2 * np][rt], v = acc[2 * np + 1][rt];
        const u32x4 d = {cvtpk(u[0], u[1]), cvtpk(u[2], u[3]), cvtpk(v[0], v[1]), cvtpk(v[2], v[3])};
        __builtin_amdgcn_raw_buffer_store_b128(d, rsS, wid * 10240 + lane * 16 + (rt * 2 + np) * 1024, 0, 16);
      }
  }
  WAITVM(0);
  BARRIER();
  STAMP(12);
  if (wid == 7) {
    unsigned long long tag = epoch + 0x9E3779B97F4A7C15ull;
    tag = (tag ^ (tag >> 30)) * 0xBF58476D1CE4E5B9ull;
    tag = (tag ^ (tag >> 27)) * 0x94D049BB133111EBull;
    tag ^= tag >> 31;
    const int lane = lane_now();
    gu64* fl = (gu64*)(ws + WS_FLAG + (b * 2 + ph) * 2048);
    if (lane == 0) __hip_atomic_store(fl + h * 16, tag, __ATOMIC_RELAXED, __HIP_MEMORY_SCOPE_AGENT);
    for (unsigned spin = 0; spin < (1u << 22); ++spin) {
      const unsigned long long v = __hip_atomic_load(fl + (lane & 15) * 16, __ATOMIC_RELAXED, __HIP_MEMORY_SCOPE_AGENT);
      if (__all(v == tag)) break;
      __builtin_amdgcn_s_sleep(1);
    }
    if (h == 0 && lane == 0) __hip_atomic_store((gu64*)(ws + WS_EPOCH + (b * 2 + ph) * 128), epoch + 1, __ATOMIC_RELAXED, __HIP_MEMORY_SCOPE_AGENT);
  }
  BARRIER();
  STAMP(13);
  {
    const int tid = wid * 64 + lane_now();
    if (tid < 280) {
      const int ci = h * 280 + tid;
      const rsrc_t rsG = __builtin_amdgcn_make_buffer_rsrc(ws + WS_SLAB + (size_t)(b * 2 + ph) * 16 * SLAB, 0, 16 * SLAB, 0x00020000);
      u32x4 v[16];
#pragma unroll
      for (int hh = 0; hh < 16; ++hh) v[hh] = __builtin_amdgcn_raw_buffer_load_b128(rsG, ci * 16, hh * SLAB, 16);
      float sm[8] = {0.f, 0.f, 0.f, 0.f, 0.f, 0.f, 0.f, 0.f};
#pragma unroll
      for (int hh = 0; hh < 16; ++hh)
#pragma unroll
        for (int i = 0; i < 4; ++i) { sm[2 * i] += __uint_as_float(v[hh][i] << 16); sm[2 * i + 1] += __uint_as_float(v[hh][i] & 0xffff0000u); }
      const int piece = ci >> 6, l = ci & 63, sl = piece / 10, rt = (piece % 10) >> 1, np = piece & 1;
      char* dst = out + ((size_t)((b * 2 + ph) * 80 + 16 * rt + (l & 15)) * DM + 64 * sl + 32 * np + 4 * (l >> 4)) * 2;
      *(u32x2*)dst = u32x2{cvtpk(sm[0], sm[1]), cvtpk(sm[2], sm[3])};
      *(u32x2*)(dst + 32) = u32x2{cvtpk(sm[4], sm[5]), cvtpk(sm[6], sm[7])};
    }
  }
}

extern "C" void kernel_launch(void* const* d_in, const int* in_sizes, int n_in, void* d_out, int out_size, void* d_ws, size_t ws_size,
                              hipStream_t stream) {
  (void)in_sizes; (void)n_in; (void)out_size; (void)ws_size;
  (void)hipFuncSetAttribute((const void*)gqa_fused, hipFuncAttributeMaxDynamicSharedMemorySize, L_TOTAL);
  gqa_fused<<<256, 512, L_TOTAL, stream>>>(d_in[0], d_in[1], d_in[2], d_in[3], d_in[4], (char*)d_ws, (char*)d_out);
}
```

```cpp
#define STAMPS
#define DIAG 0
#include <hip/hip_runtime.h>
#include <hip/hip_bf16.h>
#include <type_traits>
#define PP 1
#define QD 1
#define PRQ 2
#define PRP 0
#define ABL 0
#pragma clang diagnostic ignored "-Wint-to-pointer-cast"

typedef __bf16 bf16x8 __attribute__((ext_vector_type(8)));
typedef __bf16 bf16x4 __attribute__((ext_vector_type(4)));
typedef __bf16 bf16x2 __attribute__((ext_vector_type(2)));
typedef float f32x4 __attribute__((ext_vector_type(4)));
typedef float f32x2 __attribute__((ext_vector_type(2)));
typedef unsigned u32x4 __attribute__((ext_vector_type(4)));
typedef unsigned u32x2 __attribute__((ext_vector_type(2)));
#define LAS __attribute__((address_space(3)))
#define DI __device__ __forceinline__
typedef __amdgpu_buffer_rsrc_t rsrc_t;

constexpr int NB = 8, SQ = 160, DM = 448, HQ = 16, HKV = 8, SK = 1024, DH = 256;
constexpr unsigned XROW = DM * 2, WQROW = HQ * DH * 2, KVROW = DH * 2, WOROW = DM * 2;
constexpr unsigned SLAB = 80 * DM * 2;
constexpr unsigned WS_FLAG = 0, WS_EPOCH = 32768, WS_SLAB = 65536;
typedef __attribute__((address_space(1))) unsigned long long gu64;
constexpr unsigned KSLOT = 8448;
constexpr unsigned L_K0 = 0, L_K1 = 4 * KSLOT, L_VR = 8 * KSLOT, L_P0 = L_VR + 65536, L_P1 = L_P0 + 10240, L_MS = L_P1 + 10240, L_TOTAL = L_MS + 10240;
static_assert(L_TOTAL == 163840, "LDS");
constexpr unsigned L_FLAG = L_MS;
constexpr unsigned L_DLT = L_MS + 128;
constexpr unsigned L_OI3 = L_VR + 3 * 16384;
constexpr float QSCALE = 0.0625f * 1.4426950408889634f;
#define PTHR 65536.f
#define PLOW (1.f / 65536.f)

#define NSTAMP 14
#define STAMP_OFF (64u << 20)
#define STAMP(k) do { __builtin_amdgcn_sched_barrier(0); asm volatile("s_memrealtime %0\n\ts_waitcnt lgkmcnt(0)" : "=s"(stamp[k]) :: "memory"); __builtin_amdgcn_sched_barrier(0); } while (0)
#define ABL_KSZ(x) ((ABL & 1) ? 0 : (x))
#define ABL_VSZ(x) ((ABL & 1024) ? 0 : (x))
#define SBAR() __builtin_amdgcn_sched_barrier(0)
#define TR(k)
#define TRSAVE(cond)
#define PRIO_HI() __builtin_amdgcn_s_setprio(1)
#define PRIO_LO() __builtin_amdgcn_s_setprio(0)
#define WAITVM(n) asm volatile("s_waitcnt vmcnt(%0)" ::"i"(n) : "memory")
#define WAITLGKM0() do { asm volatile("" ::: "memory"); __builtin_amdgcn_s_waitcnt(0xC07F); asm volatile("" ::: "memory"); } while (0)
#define BARRIER() do { WAITLGKM0(); asm volatile("s_barrier" ::: "memory"); } while (0)

DI void dma(unsigned ldsaddr, unsigned voff, rsrc_t rs, unsigned soff) {
  asm volatile("s_mov_b32 m0, %0\n\ts_nop 0\n\tbuffer_load_dwordx4 %1, %2, %3 offen lds" ::"s"(ldsaddr), "v"(voff), "s"(rs), "s"(soff) : "memory");
}
DI int lane_now() { unsigned z = 0; asm volatile("" : "+v"(z)); return __builtin_amdgcn_mbcnt_hi(~0u, __builtin_amdgcn_mbcnt_lo(~0u, z)); }
DI bf16x8 lds128(unsigned a) { return *(LAS bf16x8*)a; }
DI bf16x4 ldstr(unsigned a) { return __builtin_amdgcn_ds_read_tr16_b64_v4bf16((LAS bf16x4*)a); }
DI float ldsf(unsigned a) { return *(LAS float*)a; }
DI unsigned cvtpk(float lo, float hi) { f32x2 v = {lo, hi}; return __builtin_bit_cast(unsigned, __builtin_convertvector(v, bf16x2)); }
DI f32x4 mfma(bf16x8 a, bf16x8 b, f32x4 c) { return __builtin_amdgcn_mfma_f32_16x16x32_bf16(a, b, c, 0, 0, 0); }
DI void mfma_acc(f32x4& c, bf16x8 a, bf16x8 b) { asm volatile("v_mfma_f32_16x16x32_bf16 %0, %1, %2, %0" : "+v"(c) : "v"(a), "v"(b)); }
DI void mfma_init(f32x4& d, bf16x8 a, bf16x8 b, f32x4 c) { asm volatile("v_mfma_f32_16x16x32_bf16 %0, %1, %2, %3" : "=&v"(d) : "v"(a), "v"(b), "v"(c)); }
#define MFMA_DRAIN() asm volatile("s_nop 7\n\ts_nop 1" ::: "memory")
DI float ex2(float x) { return __builtin_amdgcn_exp2f(x); }
DI unsigned off_cE(int lane) { return (unsigned)((lane & 7) ^ (((lane >> 4) & 1) << 1)) << 4; }
DI unsigned off_b(int lane) { return (lane & 15) * 128 + (((lane >> 4) ^ (((lane & 15) >> 1) & 7)) << 4); }
DI unsigned off_tr(int lane, int dt) {
  const int g = lane >> 4, qq = (lane >> 2) & 3, pp = lane & 3, f = ((qq >> 1) & 1) | ((g & 1) << 1);
  return (8 * g + qq) * 128 + ((dt ^ f) << 5) + pp * 8;
}
DI unsigned off_w(int lane, int blk) {
  const int row = lane & 15, g = lane >> 4;
  return (blk >> 1) * 5120 + (g & 1) * 512 + ((2 * (blk & 1) + (g >> 1)) * 16 + row) * 8;
}
DI bf16x8 ldsplit(unsigned a) {
  const bf16x4 lo = *(LAS bf16x4*)a, hi = *(LAS bf16x4*)(a + 512);
  return __builtin_shufflevector(lo, hi, 0, 1, 2, 3, 4, 5, 6, 7);
}
DI float xmax4(float m) { m = fmaxf(m, __shfl_xor(m, 16)); return fmaxf(m, __shfl_xor(m, 32)); }

template <int IMM> DI void dma0(unsigned ldsaddr, unsigned voff, rsrc_t rs, unsigned soff) {
  asm volatile("s_mov_b32 m0, %0\n\ts_nop 0\n\tbuffer_load_dwordx4 %1, %2, %3 offen offset:%4 lds" ::"s"(ldsaddr), "v"(voff), "s"(rs), "s"(soff), "i"(IMM) : "memory");
}
template <int IMM> DI void dmaN(unsigned voff, rsrc_t rs, unsigned soff) {
  asm volatile("buffer_load_dwordx4 %0, %1, %2 offen offset:%3 lds" ::"v"(voff), "s"(rs), "s"(soff), "i"(IMM) : "memory");
}
DI void unit_voff(unsigned (&v)[4], int lane, unsigned stride) {
  const unsigned e = (lane >> 3) * stride + off_cE(lane);
#pragma unroll
  for (int p = 0; p < 4; ++p) v[p] = ((p & 1) ? e ^ 64 : e) + p * (8 * stride - 1024);
}
DI void dma_unit(unsigned ldsaddr, const unsigned (&v)[4], rsrc_t rs, unsigned soff) {
  dma0<0>(ldsaddr, v[0], rs, soff); dmaN<1024>(v[1], rs, soff); dmaN<2048>(v[2], rs, soff); dmaN<3072>(v[3], rs, soff);
}
struct OPS { bf16x8 a[4], b[5]; };
template <bool MMA, bool READ, bool DMA, bool SPLIT = true>
DI void step(f32x4 (&acc)[4][5], const OPS& cur, OPS& nxt, const unsigned (&toff)[4], unsigned aslot, unsigned baddr,
             unsigned dslot, const unsigned (&dv)[4], rsrc_t rs, unsigned soff) {
  bf16x4 lo, hi;
  unsigned aa;
#pragma unroll
  for (int i = 0; i < 20; ++i) {
    if (MMA && !((ABL & 8) && DMA)) acc[i & 3][i >> 2] = mfma(cur.a[i & 3], cur.b[i >> 2], acc[i & 3][i >> 2]);
    if (READ && !((ABL & 4) && DMA)) {
      if (i == 0) nxt.b[0] = SPLIT ? ldsplit(baddr) : lds128(baddr);
      if (i >= 1 && i <= 8) {
        const int dt = (i - 1) >> 1;
        if ((i - 1) & 1) { hi = ldstr(aa + 512); nxt.a[dt] = __builtin_shufflevector(lo, hi, 0, 1, 2, 3, 4, 5, 6, 7); }
        else { aa = aslot + toff[dt]; lo = ldstr(aa); }
      }
      if (i >= 9 && i <= 12) nxt.b[i - 8] = SPLIT ? ldsplit(baddr + (i - 8) * 1024) : lds128(baddr + (i - 8) * 2048);
    }
    if (DMA && !(ABL & 2)) {
      if (i == 5) dma0<0>(dslot, dv[0], rs, soff);
      if (i == 9) dmaN<1024>(dv[1], rs, soff);
      if (i == 13) dmaN<2048>(dv[2], rs, soff);
      if (i == 17) dmaN<3072>(dv[3], rs, soff);
    }
    SBAR();
  }
}
DI void read_b(OPS& o, unsigned addr) {
#pragma unroll
  for (int rt = 0; rt < 5; ++rt) o.b[rt] = ldsplit(addr + rt * 1024);
}
DI void zero_acc(f32x4 (&acc)[4][5]) {
#pragma unroll
  for (int dt = 0; dt < 4; ++dt)
#pragma unroll
    for (int rt = 0; rt < 5; ++rt) acc[dt][rt] = f32x4{0.f, 0.f, 0.f, 0.f};
}
DI void write_img(unsigned img, const f32x4 (&acc)[4][5], const float (&sc)[5], int lane) {
#pragma unroll
  for (int dt = 0; dt < 4; ++dt) {
    const unsigned a = img + off_w(lane, dt);
#pragma unroll
    for (int rt = 0; rt < 5; ++rt) {
      u32x2 w = {cvtpk(acc[dt][rt][0] * sc[rt], acc[dt][rt][1] * sc[rt]), cvtpk(acc[dt][rt][2] * sc[rt], acc[dt][rt][3] * sc[rt])};
      *(LAS u32x2*)(a + rt * 1024) = w;
    }
  }
}
DI float read_D(int rt, unsigned fl, int row) {
  float d[4];
#pragma unroll
  for (int w = 0; w < 4; ++w) {
    const float v = ldsf(L_DLT + (w * 80 + rt * 16 + row) * 4);
    d[w] = ((fl >> (8 * w + rt)) & 1) ? v : 0.f;
  }
  return fmaxf(fmaxf(d[0], d[1]), fmaxf(d[2], d[3]));
}
DI float fadd_s(float a, float b) { float r = a + b; asm("" : "+v"(r)); return r; }
DI unsigned ls_addr(int w, int rt, int row) { return (rt < 4 ? L_K0 + rt * 64 : L_K1) + w * KSLOT + 8192 + row * 4; }

constexpr unsigned long long mkord(const int (&o)[16]) { unsigned long long r = 0; for (int i = 0; i < 16; ++i) r |= (unsigned long long)o[i] << (4 * i); return r; }
constexpr int ORD0[16] = {0, 4, 1, 2, 3, 8, 5, 6, 7, 12, 9, 10, 11, 13, 14, 15};
constexpr int ORD1[16] = {1, 0, 5, 2, 3, 4, 9, 6, 7, 8, 13, 10, 11, 12, 14, 15};
constexpr int ORD2[16] = {2, 0, 1, 6, 3, 4, 5, 10, 7, 8, 9, 14, 11, 12, 13, 15};
constexpr int ORD3[16] = {3, 0, 1, 2, 7, 4, 5, 6, 11, 8, 9, 10, 15, 12, 13, 14};

__global__ __launch_bounds__(512, 2) void gqa_fused(const void* __restrict__ x, const void* __restrict__ kc, const void* __restrict__ vc,
                                                    const void* __restrict__ wq, const void* __restrict__ wo, char* ws, char* __restrict__ out) {
  unsigned long long stamp[NSTAMP] = {};
  STAMP(0);
  unsigned long long cyc0 = __builtin_amdgcn_s_memtime(), cycB = 0;
  const int wid = __builtin_amdgcn_readfirstlane(threadIdx.x >> 6);
  const int bid = blockIdx.x, kvh = bid & 7, q = (bid >> 3) & 3, b = bid >> 5, hg = q >> 1, ph = q & 1, h = 2 * kvh + hg;
  const unsigned rotA = (unsigned)(2 * b + ph) % 7u, rotC = (unsigned)(2 * b + ph) & 3u;
  const unsigned long long ord = q == 0 ? mkord(ORD0) : q == 1 ? mkord(ORD1) : q == 2 ? mkord(ORD2) : mkord(ORD3);
#define TILE(t) ((ABL & 4096) ? (unsigned)(t) : ((unsigned)(ord >> (4 * (t))) & 15u))
  const rsrc_t rsWO = __builtin_amdgcn_make_buffer_rsrc((char*)wo + (size_t)h * DH * WOROW, 0, wid == 7 ? 0 : DH * WOROW, 0x00020000);
  const rsrc_t rsV = __builtin_amdgcn_make_buffer_rsrc((char*)vc + (size_t)(((ABL & 8192) ? 0 : b) * HKV + kvh) * SK * KVROW, 0, ABL_VSZ(SK * KVROW), 0x00020000);
  const int w = wid & 3;
  const unsigned ks0 = L_K0 + w * KSLOT, ks1 = L_K1 + w * KSLOT, vra = L_VR + w * 16384, vrb = vra + 8192;
  unsigned hb0, hb1;
  unsigned long long epoch = 0;

  if (wid < 4) {
    const rsrc_t rsK = __builtin_amdgcn_make_buffer_rsrc((char*)kc + (size_t)(((ABL & 8192) ? 0 : b) * HKV + kvh) * SK * KVROW - 128, 0, ABL_KSZ(SK * KVROW + 128), 0x00020000);
    unsigned kv0, kra, pwa;
    {
      const int lane = lane_now(), row = lane & 15, g = lane >> 4;
      kv0 = (lane >> 5) * 512 + (((lane & 31) ^ (lane >> 5)) << 4);
      kra = ks0 + (row >> 1) * 1056 + (row & 1) * 512 + ((g ^ (row & 1)) << 4);
      pwa = L_P0 + off_w(lane, w);
    }
#define DMAK(P, slot, so)                                                                     \
  do {                                                                                        \
    if (((P) & 3) == 0) dma0<0>((slot) + ((P) >> 2) * 4224, kv0, rsK, (so) + ((P) >> 2) * 4096); \
    else dmaN<((P) & 3) * 1056>(kv0, rsK, (so) + ((P) >> 2) * 4096 - ((P) & 3) * 32);           \
  } while (0)
    auto dmaK = [&](unsigned slot, unsigned tile) __attribute__((always_inline)) {
      const unsigned so = tile * 32768 + w * 8192 + 128;
      DMAK(0, slot, so); DMAK(1, slot, so); DMAK(2, slot, so); DMAK(3, slot, so);
      DMAK(4, slot, so); DMAK(5, slot, so); DMAK(6, slot, so); DMAK(7, slot, so);
    };
    bf16x8 kf[8];
    {
      const rsrc_t rsX = __builtin_amdgcn_make_buffer_rsrc((char*)x + (size_t)(b * SQ + ph * 80) * XROW, 0, 80 * XROW, 0x00020000);
      const rsrc_t rsWQ = __builtin_amdgcn_make_buffer_rsrc((char*)wq, 0, DM * WQROW, 0x00020000);
      const int lane = lane_now(), r8 = lane >> 3, ch = lane & 7;
      unsigned wv[4], xv[3];
      unit_voff(wv, lane, WQROW);
#pragma unroll
      for (int i = 0; i < 3; ++i) { const int o = i == 2 ? 12 : 8 * i, r = 20 * w + o + r8; xv[i] = r * XROW + ((ch ^ ((r >> 1) & 7)) << 4) - o * 128; }
      unsigned cc = rotA, as0 = vrb, as1 = vra, as2 = ks1, xs0 = L_P0, xs1 = L_P1, xs2 = L_MS;
      auto issue = [&]() __attribute__((always_inline)) {
        dma0<0>(xs0 + 20 * w * 128, xv[0], rsX, cc * 128); dmaN<1024>(xv[1], rsX, cc * 128); dmaN<1536>(xv[2], rsX, cc * 128);
        const unsigned so = cc * 64 * WQROW + h * 512 + w * 128;
        dma_unit(as0, wv, rsWQ, so);
        dma_unit(as0 + 4096, wv, rsWQ, so + 32 * WQROW);
        cc = cc == 6 ? 0 : cc + 1;
        unsigned t0 = as0; as0 = as1; as1 = as2; as2 = t0;
        t0 = xs0; xs0 = xs1; xs1 = xs2; xs2 = t0;
      };
      issue();
#pragma unroll 1
      for (int s = 0; s < 6; ++s) {
        issue();
        WAITVM(11);
        BARRIER();
        if (s == 0) STAMP(1);
      }
      dmaK(ks0, TILE(0));
      unit_voff(wv, lane, KVROW);
      dma_unit(vra, wv, rsV, TILE(0) * 32768 + w * 128);
      dma_unit(vra + 4096, wv, rsV, TILE(0) * 32768 + 16384 + w * 128);
      WAITVM(16);
      BARRIER();
    }
    WAITVM(8);
#pragma unroll
    for (int ks = 0; ks < 8; ++ks) kf[ks] = lds128(kra + ks * 64);
    if (w == 0) { const int lane = lane_now(); if (lane < 32) *(LAS unsigned*)(L_FLAG + lane * 4) = 0u; }
    WAITLGKM0();
    dmaK(ks0, TILE(1));
    BARRIER();
    STAMP(2);
    bf16x8 qf[5][8];
    {
      const unsigned l8 = lane_now() * 8;
#pragma unroll
      for (int rt = 0; rt < 5; ++rt)
#pragma unroll
        for (int ks = 0; ks < 8; ++ks) {
          const unsigned img = (ks >> 1) == 3 ? L_P1 : L_K1 + (ks >> 1) * 10240;
          qf[rt][ks] = ldsplit(img + (ks & 1) * 5120 + rt * 1024 + l8);
        }
    }
    f32x4 negm[5];
    float lsum[5];
#pragma unroll
    for (int rt = 0; rt < 5; ++rt) { negm[rt] = f32x4{0.f, 0.f, 0.f, 0.f}; lsum[rt] = 0.f; }
    f32x4 S0 = negm[0];
#pragma unroll
    for (int ks = 0; ks < 8; ++ks) S0 = mfma(kf[ks], qf[0][ks], S0);
    WAITVM(8);
    BARRIER();
    auto fixup = [&](unsigned fl, unsigned pprev, f32x4& Sa, f32x4& Sb) __attribute__((always_inline)) {
      const int row = lane_now() & 15;
#pragma unroll
      for (int rt = 0; rt < 5; ++rt) {
        const float D = read_D(rt, fl, row);
        const float own = ((fl >> (8 * w + rt)) & 1) ? ldsf(L_DLT + (w * 80 + rt * 16 + row) * 4) : 0.f;
        const float f = ex2(own - D);
        u32x2 pk = *(LAS u32x2*)(pprev + rt * 1024);
        const float a0 = __uint_as_float(pk[0] << 16) * f, a1 = __uint_as_float(pk[0] & 0xffff0000u) * f;
        const float a2 = __uint_as_float(pk[1] << 16) * f, a3 = __uint_as_float(pk[1] & 0xffff0000u) * f;
        *(LAS u32x2*)(pprev + rt * 1024) = u32x2{cvtpk(a0, a1), cvtpk(a2, a3)};
        lsum[rt] *= f;
        negm[rt] -= D;
        if (rt == 0) Sa -= D;
        if (rt == 1) Sb -= D;
        SBAR();
      }
      BARRIER();
    };
    unsigned dslot = ks1;
    const unsigned pwsum = 2 * pwa + 10240, krsum = 2 * kra + L_K1;
    auto qk_tile = [&](int t, auto wo_c) __attribute__((always_inline)) {
      constexpr bool WO = decltype(wo_c)::value;
      unsigned so, wv[4];
      if constexpr (WO) { unit_voff(wv, lane_now(), WOROW); so = ((rotC + t - 14) & 3) * 64 * WOROW + w * 128; }
      else so = TILE(t + 2) * 32768 + w * 8192 + 128;
      unsigned fl = *(LAS unsigned*)(L_FLAG + t * 4);
      f32x4 S1;
      float p0, p1, p2, p3, ts, tm;
      u32x2 pk;
#define QDMA(P)                                                                                                   \
  do {                                                                                                            \
    if constexpr (WO) {                                                                                           \
      if (((P) & 3) == 0) dma0<0>(dslot + ((P) >> 2) * 4096, wv[0], rsWO, so + ((P) >> 2) * 32 * WOROW);           \
      else dmaN<((P) & 3) * 1024>(wv[(P) & 3], rsWO, so + ((P) >> 2) * 32 * WOROW);                                \
    } else if (!(ABL & 2048)) DMAK(P, dslot, so);                                                                                    \
  } while (0)
#define QM(i, RN, SN)                                                                                              \
  if (ABL & 16) { SN = negm[RN]; asm volatile("" : "+v"(SN) : "v"(kf[i]), "v"(qf[RN][i])); }                       \
  else if ((i) == 0) mfma_init(SN, kf[i], qf[RN][i], negm[RN]); else mfma_acc(SN, kf[i], qf[RN][i]);
#define QKBLOCK(RC, SC, RN, SN)                                                                                    \
  TR(2 * RC); QM(0, RN, SN) SBAR(); PRIO_HI(); p0 = ex2(SC[0]); SBAR();                                                        \
  QM(1, RN, SN) SBAR(); p1 = ex2(SC[1]); SBAR();                                                                   \
  QM(2, RN, SN) SBAR(); p2 = ex2(SC[2]); SBAR();                                                                   \
  QM(3, RN, SN) SBAR(); p3 = ex2(SC[3]); SBAR();                                                                   \
  QM(4, RN, SN) SBAR(); ts = fadd_s(p0, p1); pk[0] = cvtpk(p0, p1); SBAR();                                        \
  QM(5, RN, SN) SBAR(); p0 = fadd_s(p2, p3); pk[1] = cvtpk(p2, p3); SBAR();                                        \
  QM(6, RN, SN) SBAR(); ts = fadd_s(ts, p0); if (RC == 0) fl = __builtin_amdgcn_readfirstlane(fl); SBAR();         \
  QM(7, RN, SN) SBAR(); PRIO_LO(); TR(2 * RC + 1);                                                                                \
  if (RC == 0) {                                                                                                   \
    if (__builtin_expect(fl != 0, 0)) {                                                                            \
      MFMA_DRAIN();                                                                                                \
      fixup(fl, pwsum - pwa, SC, SN);                                                                              \
      p0 = ex2(SC[0]); p1 = ex2(SC[1]); p2 = ex2(SC[2]); p3 = ex2(SC[3]);                                          \
      ts = (p0 + p1) + (p2 + p3);                                                                                  \
      pk = u32x2{cvtpk(p0, p1), cvtpk(p2, p3)};                                                                    \
    }                                                                                                              \
    SBAR();                                                                                                        \
  }                                                                                                                \
  if (RC == 3) {                                                                                                   \
    TR(13); WAITVM(6); TR(14);                                                                                     \
    _Pragma("unroll") for (int ks = 0; ks < 8; ++ks) kf[ks] = lds128(kra + ks * 64);                               \
    SBAR();                                                                                                        \
  }                                                                                                                \
  lsum[RC] = fadd_s(lsum[RC], ts); tm = RC == 0 ? ts : fadd_s(tm, ts);                                             \
  *(LAS u32x2*)(pwa + RC * 1024) = pk; SBAR();
      SBAR();
      QKBLOCK(0, S0, 1, S1)
      QDMA(0); QDMA(1); SBAR();
      QKBLOCK(1, S1, 2, S0)
      QDMA(2); QDMA(3); SBAR();
      QKBLOCK(2, S0, 3, S1)
      QDMA(4); QDMA(5); SBAR();
      QKBLOCK(3, S1, 4, S0)
      QKBLOCK(4, S0, 0, S1)
      QDMA(6); QDMA(7); SBAR();
      S0 = S1;
#undef QKBLOCK
#undef QM
#undef QDMA
      bool bad = !(tm <= PTHR);
      if (t == 0) bad |= !(fminf(fminf(fminf(lsum[0], lsum[1]), fminf(lsum[2], lsum[3])), lsum[4]) >= PLOW);
      if (!(ABL & 128)) if (__builtin_expect(__any(bad), 0)) {
        const int lane = lane_now();
#pragma unroll
        for (int ks = 0; ks < 8; ++ks)
          kf[ks] = __builtin_bit_cast(bf16x8, __builtin_amdgcn_raw_buffer_load_b128(rsK, (lane & 15) * 512 + (lane >> 4) * 16 + ks * 64, TILE(t) * 32768 + w * 8192 + 128, 0));
#pragma unroll
        for (int rt = 0; rt < 5; ++rt) {
          f32x4 S = negm[rt];
#pragma unroll
          for (int ks = 0; ks < 8; ++ks) S = mfma(kf[ks], qf[rt][ks], S);
          float dl = ceilf(xmax4(fmaxf(fmaxf(S[0], S[1]), fmaxf(S[2], S[3]))));
          if (t > 0) dl = fmaxf(dl, 0.f);
          p0 = ex2(S[0] - dl); p1 = ex2(S[1] - dl); p2 = ex2(S[2] - dl); p3 = ex2(S[3] - dl);
          lsum[rt] = (t > 0 && lsum[rt] <= 1.0e18f) ? lsum[rt] * ex2(-dl) : (p0 + p1) + (p2 + p3);
          *(LAS u32x2*)(pwa + rt * 1024) = u32x2{cvtpk(p0, p1), cvtpk(p2, p3)};
          if (lane < 16) *(LAS float*)(L_DLT + (w * 80 + rt * 16 + lane) * 4) = dl;
          SBAR();
        }
#pragma unroll
        for (int ks = 0; ks < 8; ++ks) kf[ks] = lds128(kra + ks * 64);
        *(LAS unsigned char*)(L_FLAG + (t + 1) * 4 + w) = 31;
      }
      kra = krsum - kra;
      pwa = pwsum - pwa;
      dslot = ks0 + ks1 - dslot;
      TR(10);
      WAITLGKM0();
      TR(11);
      BARRIER();
      TR(12);
      TRSAVE(t == 7);
    };
#pragma unroll 1
    for (int t = 0; t < 14; ++t) {
      qk_tile(t, std::false_type{});
      if (t == 0) { STAMP(3); cycB = __builtin_amdgcn_s_memtime(); }
      if (t == 12) { STAMP(4); stamp[10] = stamp[0] + (__builtin_amdgcn_s_memtime() - cycB) / 120; }
    }
#pragma unroll 1
    for (int t = 14; t < 16; ++t) qk_tile(t, std::true_type{});
    {
      const unsigned fl = __builtin_amdgcn_readfirstlane(*(LAS unsigned*)(L_FLAG + 64));
      f32x4 dummy = {0.f, 0.f, 0.f, 0.f};
      if (__builtin_expect(fl != 0, 0)) fixup(fl, pwsum - pwa, dummy, dummy);
    }
    {
      const int lane = lane_now();
#pragma unroll
      for (int rt = 0; rt < 5; ++rt) {
        float l = lsum[rt];
        l += __shfl_xor(l, 16);
        l += __shfl_xor(l, 32);
        if (lane < 16) *(LAS float*)ls_addr(w, rt, lane) = l;
      }
    }
    BARRIER();
    STAMP(5);
    hb0 = ks1; hb1 = ks0;
  } else {
    if (wid == 7) {
      const unsigned long long e = __hip_atomic_load((gu64*)(ws + WS_EPOCH + (b * 2 + ph) * 128), __ATOMIC_RELAXED, __HIP_MEMORY_SCOPE_AGENT);
      epoch = ((unsigned long long)__builtin_amdgcn_readfirstlane((unsigned)(e >> 32)) << 32) | __builtin_amdgcn_readfirstlane((unsigned)e);
    }
    const int lane = lane_now(), row = lane & 15;
    const unsigned xoff = off_b(lane), xoff1 = xoff ^ 64, boff = lane * 8, boff1 = boff + 5120;
    unsigned toff[4], dv[4];
#pragma unroll
    for (int dt = 0; dt < 4; ++dt) toff[dt] = off_tr(lane, dt);
    unit_voff(dv, lane, KVROW);
    f32x4 acc[4][5];
    zero_acc(acc);
    OPS o0, o1;
    {
      unsigned as0 = vrb, as1 = vra, as2 = ks1, xs0 = L_P0, xs1 = L_P1, xs2 = L_MS;
      BARRIER();
      STAMP(1);
      step<false, true, false, false>(acc, o1, o0, toff, as0, xs0 + xoff, 0, dv, rsV, 0);
#pragma unroll 1
      for (int s = 0; s < 6; ++s) {
        step<true, true, false, false>(acc, o0, o1, toff, as0 + 4096, xs0 + xoff1, 0, dv, rsV, 0);
        unsigned t0 = as0; as0 = as1; as1 = as2; as2 = t0;
        t0 = xs0; xs0 = xs1; xs1 = xs2; xs2 = t0;
        BARRIER();
        step<true, true, false, false>(acc, o1, o0, toff, as0, xs0 + xoff, 0, dv, rsV, 0);
      }
      step<true, true, false, false>(acc, o0, o1, toff, as0 + 4096, xs0 + xoff1, 0, dv, rsV, 0);
      step<true, false, false>(acc, o1, o0, toff, 0, 0, 0, dv, rsV, 0);
    }
    {
      const float sc[5] = {QSCALE, QSCALE, QSCALE, QSCALE, QSCALE};
      write_img(w == 3 ? L_P1 : L_K1 + w * 10240, acc, sc, lane);
    }
    BARRIER();
    STAMP(2);
    dma_unit(vrb, dv, rsV, TILE(1) * 32768 + w * 128);
    dma_unit(vrb + 4096, dv, rsV, TILE(1) * 32768 + 16384 + w * 128);
    zero_acc(acc);
    BARRIER();
    bf16x8 va[2][4], vb[2][4];
#pragma unroll
    for (int hh = 0; hh < 2; ++hh)
#pragma unroll
      for (int dt = 0; dt < 4; ++dt) {
        const bf16x4 lo = ldstr(vra + toff[dt] + hh * 4096), hi = ldstr(vra + toff[dt] + hh * 4096 + 512);
        va[hh][dt] = __builtin_shufflevector(lo, hi, 0, 1, 2, 3, 4, 5, 6, 7);
      }
    unsigned ta[4];
#pragma unroll
    for (int dt = 0; dt < 4; ++dt) ta[dt] = vra + toff[dt];
    unsigned pa = L_P0 + boff;
    asm volatile("" : "+v"(pa));
    rsrc_t rsD = rsV;
    unsigned dstep = 16384;
    auto pv_tile = [&](int t, auto par_c, bf16x8 (&vc)[2][4], bf16x8 (&vn)[2][4]) __attribute__((always_inline)) {
      constexpr int PAR = decltype(par_c)::value;
      unsigned so;
      if (t < 14) so = TILE(t + 2) * 32768 + w * 128;
      else so = ((rotC + t - 14) & 3) * 64 * WOROW + wid * 128;
      const unsigned dsl = vra + PAR * 8192;
      bf16x8 pf[5][2];
#define RDP(rt) { pf[rt][0] = ldsplit(pa + PAR * 10240 + (rt) * 1024); pf[rt][1] = ldsplit(pa + PAR * 10240 + 5120 + (rt) * 1024); }
#define PM(i, rt) if (!(ABL & 8)) mfma_acc(acc[(i) & 3][rt], vc[(i) >> 2][(i) & 3], pf[rt][(i) >> 2]);
#define RDV(dt, hh)                                                                                                  \
  vn[hh][dt] = __builtin_shufflevector(ldstr(ta[dt] + (PAR ^ 1) * 8192 + (hh) * 4096), ldstr(ta[dt] + (PAR ^ 1) * 8192 + (hh) * 4096 + 512), 0, 1, 2, 3, 4, 5, 6, 7);
#define BURST(rt)                                                                                                  \
  SBAR(); TR(2 * rt + 1); PM(0, rt) SBAR(); PRIO_HI(); SBAR(); PM(1, rt) PM(2, rt) PM(3, rt) PM(4, rt) PM(5, rt) PM(6, rt) PM(7, rt) SBAR(); PRIO_LO(); TR(2 * rt + 2); SBAR();
#define PD(P)                                                                                                      \
  if (!(ABL & 2)) {                                                                                                \
    if (((P) & 3) == 0) dma0<0>(dsl + ((P) >> 2) * 4096, dv[0], rsD, (P) ? so + dstep : so);                       \
    else dmaN<((P) & 3) * 1024>(dv[(P) & 3], rsD, (P) >> 2 ? so + dstep : so);                                     \
  }
      TR(11);
      BARRIER();
      TR(12);
      TRSAVE(t == 7);
      TR(0);
      unsigned fl = *(LAS unsigned*)(L_FLAG + (t + 1) * 4);
      RDP(0) RDP(1)
      SBAR();
      PD(0)
      SBAR();
      fl = __builtin_amdgcn_readfirstlane(fl);
      if (__builtin_expect(fl != 0, 0)) {
        if (t > 0) {
#pragma unroll
          for (int rt = 0; rt < 5; ++rt) {
            const float f = ex2(-read_D(rt, fl, row));
#pragma unroll
            for (int dt = 0; dt < 4; ++dt) acc[dt][rt] *= f;
          }
        }
        BARRIER();
        RDP(0) RDP(1)
      }
      BURST(0)
      RDP(2) SBAR();
      PD(1) PD(2)
      BURST(1)
      RDP(3) SBAR();
      WAITVM(7);
      RDV(0, 0) RDV(1, 0) SBAR();
      PD(3)
      BURST(2)
      RDP(4) RDV(2, 0) RDV(3, 0) SBAR();
      PD(4) PD(5)
      BURST(3)
      TR(13);
      WAITVM(6);
      RDV(0, 1) RDV(1, 1) RDV(2, 1) RDV(3, 1) SBAR();
      PD(6) PD(7)
      if (PAR == 1 && t == 15) BARRIER();
      BURST(4)
#undef PD
#undef BURST
#undef RDV
#undef PM
#undef RDP
    };
#pragma unroll 1
    for (int t = 0; t < 16; t += 2) {
      if (t == 14) { unit_voff(dv, lane_now(), WOROW); rsD = rsWO; dstep = 32 * WOROW; }
      pv_tile(t, std::integral_constant<int, 0>{}, va, vb);
      pv_tile(t + 1, std::integral_constant<int, 1>{}, vb, va);
      if (t == 10) STAMP(4);
    }
    MFMA_DRAIN();
    STAMP(5);
    float inv[5];
#pragma unroll
    for (int rt = 0; rt < 5; ++rt) {
      float l = 0.f;
#pragma unroll
      for (int ww = 0; ww < 4; ++ww) l += ldsf(ls_addr(ww, rt, row));
      inv[rt] = 1.f / l;
    }
    if (w == 3) WAITVM(0);
    write_img(w == 0 ? L_P0 : w == 1 ? L_P1 : w == 2 ? L_MS : L_OI3, acc, inv, lane);
    hb0 = vra; hb1 = vrb;
  }

  BARRIER();
  STAMP(6);
  if (wid < 7) {
    const int lane = lane_now();
    const unsigned boff = lane * 8, boff1 = boff + 5120;
    unsigned toff[4], dv[4];
#pragma unroll
    for (int dt = 0; dt < 4; ++dt) toff[dt] = off_tr(lane, dt);
    unit_voff(dv, lane, WOROW);
    f32x4 acc[4][5];
    zero_acc(acc);
    OPS o0, o1;
    auto imgof = [&](unsigned cc) __attribute__((always_inline)) { cc &= 3; return cc == 0 ? L_P0 : cc == 1 ? L_P1 : cc == 2 ? L_MS : L_OI3; };
    WAITVM(12);
    step<false, true, false>(acc, o1, o0, toff, hb0, imgof(rotC) + boff, 0, dv, rsWO, 0);
#pragma unroll 1
    for (int s = 0; s < 2; ++s) {
      const unsigned so = ((rotC + s + 2) & 3) * 64 * WOROW + wid * 128;
      WAITVM(8);
      step<true, true, true>(acc, o0, o1, toff, hb0 + 4096, imgof(rotC + s) + boff1, hb0, dv, rsWO, so);
      WAITVM(8);
      step<true, true, true>(acc, o1, o0, toff, hb1, imgof(rotC + s + 1) + boff, hb0 + 4096, dv, rsWO, so + 32 * WOROW);
      const unsigned t0 = hb0; hb0 = hb1; hb1 = t0;
    }
    WAITVM(8);
    step<true, true, false>(acc, o0, o1, toff, hb0 + 4096, imgof(rotC + 2) + boff1, 0, dv, rsWO, 0);
    WAITVM(4);
    step<true, true, false>(acc, o1, o0, toff, hb1, imgof(rotC + 3) + boff, 0, dv, rsWO, 0);
    WAITVM(0);
    step<true, true, false>(acc, o0, o1, toff, hb1 + 4096, imgof(rotC + 3) + boff1, 0, dv, rsWO, 0);
    step<true, false, false>(acc, o1, o0, toff, 0, 0, 0, dv, rsWO, 0);
    STAMP(7);
    const rsrc_t rsS = __builtin_amdgcn_make_buffer_rsrc(ws + WS_SLAB + (size_t)((b * 2 + ph) * 16 + h) * SLAB, 0, SLAB, 0x00020000);
#pragma unroll
    for (int rt = 0; rt < 5; ++rt)
#pragma unroll
      for (int np = 0; np < 2; ++np) {
        const f32x4 u = acc[2 * np][rt], v = acc[2 * np + 1][rt];
        const u32x4 d = {cvtpk(u[0], u[1]), cvtpk(u[2], u[3]), cvtpk(v[0], v[1]), cvtpk(v[2], v[3])};
        __builtin_amdgcn_raw_buffer_store_b128(d, rsS, wid * 10240 + lane * 16 + (rt * 2 + np) * 1024, 0, 16);
      }
  }
  WAITVM(0);
  BARRIER();
  STAMP(12);
  if (wid == 7) {
    unsigned long long tag = epoch + 0x9E3779B97F4A7C15ull;
    tag = (tag ^ (tag >> 30)) * 0xBF58476D1CE4E5B9ull;
    tag = (tag ^ (tag >> 27)) * 0x94D049BB133111EBull;
    tag ^= tag >> 31;
    const int lane = lane_now();
    gu64* fl = (gu64*)(ws + WS_FLAG + (b * 2 + ph) * 2048);
    if (lane == 0) __hip_atomic_store(fl + h * 16, tag, __ATOMIC_RELAXED, __HIP_MEMORY_SCOPE_AGENT);
    for (unsigned spin = 0; spin < (1u << 22); ++spin) {
      const unsigned long long v = __hip_atomic_load(fl + (lane & 15) * 16, __ATOMIC_RELAXED, __HIP_MEMORY_SCOPE_AGENT);
      if (__all(v == tag)) break;
      __builtin_amdgcn_s_sleep(1);
    }
    if (h == 0 && lane == 0) __hip_atomic_store((gu64*)(ws + WS_EPOCH + (b * 2 + ph) * 128), epoch + 1, __ATOMIC_RELAXED, __HIP_MEMORY_SCOPE_AGENT);
  }
  BARRIER();
  STAMP(13);
  {
    const int tid = wid * 64 + lane_now();
    if (tid < 280) {
      const int ci = h * 280 + tid;
      const rsrc_t rsG = __builtin_amdgcn_make_buffer_rsrc(ws + WS_SLAB + (size_t)(b * 2 + ph) * 16 * SLAB, 0, 16 * SLAB, 0x00020000);
      u32x4 v[16];
#pragma unroll
      for (int hh = 0; hh < 16; ++hh) v[hh] = __builtin_amdgcn_raw_buffer_load_b128(rsG, ci * 16, hh * SLAB, 16);
      float sm[8] = {0.f, 0.f, 0.f, 0.f, 0.f, 0.f, 0.f, 0.f};
#pragma unroll
      for (int hh = 0; hh < 16; ++hh)
#pragma unroll
        for (int i = 0; i < 4; ++i) { sm[2 * i] += __uint_as_float(v[hh][i] << 16); sm[2 * i + 1] += __uint_as_float(v[hh][i] & 0xffff0000u); }
      const int piece = ci >> 6, l = ci & 63, sl = piece / 10, rt = (piece % 10) >> 1, np = piece & 1;
      char* dst = out + ((size_t)((b * 2 + ph) * 80 + 16 * rt + (l & 15)) * DM + 64 * sl + 32 * np + 4 * (l >> 4)) * 2;
      *(u32x2*)dst = u32x2{cvtpk(sm[0], sm[1]), cvtpk(sm[2], sm[3])};
      *(u32x2*)(dst + 32) = u32x2{cvtpk(sm[4], sm[5]), cvtpk(sm[6], sm[7])};
    }
  }
  WAITVM(0);
  STAMP(8);
  if (wid == 0) {
    unsigned long long d = DIAG == 99 ? (__builtin_amdgcn_s_memtime() - cyc0) / 20 : DIAG == 0 ? 0 : 2 * (stamp[DIAG == 99 ? 0 : DIAG] - stamp[0]);
    while (__builtin_amdgcn_s_memrealtime() - stamp[8] < d) __builtin_amdgcn_s_sleep(1);
  }
}

extern "C" void kernel_launch(void* const* d_in, const int* in_sizes, int n_in, void* d_out, int out_size, void* d_ws, size_t ws_size,
                              hipStream_t stream) {
  (void)in_sizes; (void)n_in; (void)out_size; (void)ws_size;
  (void)hipFuncSetAttribute((const void*)gqa_fused, hipFuncAttributeMaxDynamicSharedMemorySize, L_TOTAL);
  gqa_fused<<<256, 512, L_TOTAL, stream>>>(d_in[0], d_in[1], d_in[2], d_in[3], d_in[4], (char*)d_ws, (char*)d_out);
}
```

```cpp
#include <hip/hip_runtime.h>
#include <hip/hip_bf16.h>
#include <type_traits>
#define PP 1
#define SPT 1
#define SPCQ 1
#define SPCP 1
#define TRT 7
#define SLP 4
#define TOUCH 2
#define PLAY 0
#define CPF 1
#define ABL 0
#pragma clang diagnostic ignored "-Wint-to-pointer-cast"

typedef __bf16 bf16x8 __attribute__((ext_vector_type(8)));
typedef __bf16 bf16x4 __attribute__((ext_vector_type(4)));
typedef __bf16 bf16x2 __attribute__((ext_vector_type(2)));
typedef float f32x4 __attribute__((ext_vector_type(4)));
typedef float f32x2 __attribute__((ext_vector_type(2)));
typedef unsigned u32x4 __attribute__((ext_vector_type(4)));
typedef unsigned u32x2 __attribute__((ext_vector_type(2)));
#define LAS __attribute__((address_space(3)))
#define DI __device__ __forceinline__
typedef __amdgpu_buffer_rsrc_t rsrc_t;

constexpr int NB = 8, SQ = 160, DM = 448, HQ = 16, HKV = 8, SK = 1024, DH = 256;
constexpr unsigned XROW = DM * 2, WQROW = HQ * DH * 2, KVROW = DH * 2, WOROW = DM * 2;
constexpr unsigned SLAB = 80 * DM * 2;
constexpr unsigned WS_FLAG = 0, WS_EPOCH = 32768, WS_SLAB = 65536;
typedef __attribute__((address_space(1))) unsigned long long gu64;
constexpr unsigned KSLOT = 8448;
constexpr unsigned L_K0 = 0, L_K1 = 4 * KSLOT, L_VR = 8 * KSLOT, L_P0 = L_VR + 65536, L_P1 = L_P0 + 10240, L_MS = L_P1 + 10240, L_TOTAL = L_MS + 10240;
static_assert(L_TOTAL == 163840, "LDS");
constexpr unsigned L_FLAG = L_MS;
constexpr unsigned L_DLT = L_MS + 128;
constexpr unsigned L_OI3 = L_VR + 3 * 16384;
constexpr int CODE_BYTES = 24192;
constexpr float QSCALE = 0.0625f * 1.4426950408889634f;
#define PTHR 65536.f
#define GTHR 12.f
#define PLOW (1.f / 65536.f)

#define STAMP(k)
#define ABL_KSZ(x) ((ABL & 1) ? 0 : (x))
#define ABL_VSZ(x) ((ABL & 1024) ? 0 : (x))
#define SBAR() __builtin_amdgcn_sched_barrier(0)
#define TRX(k) do { SBAR(); asm volatile("s_memtime %0" : "=s"(tr[k])); SBAR(); } while (0)
#define TRXSAVE(cond) do { if (cond) { WAITLGKM0(); if (lane_now() == 0) for (int k_ = 0; k_ < 16; ++k_) ((unsigned long long*)(ws + (80u << 20)))[(bid * 8 + wid) * 16 + k_] = tr[k_]; } } while (0)
#define TR(k)
#define TRSAVE(cond)
#define TB(k)
#define TBSAVE(cond)
#define TA(k)
#define TASAVE(cond)
#define PRIO_HI() __builtin_amdgcn_s_setprio(1)
#define PRIO_LO() __builtin_amdgcn_s_setprio(0)
#define WAITVM(n) asm volatile("s_waitcnt vmcnt(%0)" ::"i"(n) : "memory")
#define WAITLGKM0() do { asm volatile("" ::: "memory"); __builtin_amdgcn_s_waitcnt(0xC07F); asm volatile("" ::: "memory"); } while (0)
#define BARRIER() do { WAITLGKM0(); asm volatile("s_barrier" ::: "memory"); } while (0)

DI int lane_now() { unsigned z = 0; asm volatile("" : "+v"(z)); return __builtin_amdgcn_mbcnt_hi(~0u, __builtin_amdgcn_mbcnt_lo(~0u, z)); }
DI bf16x8 lds128(unsigned a) { return *(LAS bf16x8*)a; }
DI bf16x4 ldstr(unsigned a) { return __builtin_amdgcn_ds_read_tr16_b64_v4bf16((LAS bf16x4*)a); }
DI float ldsf(unsigned a) { return *(LAS float*)a; }
DI unsigned cvtpk(float lo, float hi) { f32x2 v = {lo, hi}; return __builtin_bit_cast(unsigned, __builtin_convertvector(v, bf16x2)); }
DI f32x4 mfma(bf16x8 a, bf16x8 b, f32x4 c) { return __builtin_amdgcn_mfma_f32_16x16x32_bf16(a, b, c, 0, 0, 0); }
DI void mfma_acc(f32x4& c, bf16x8 a, bf16x8 b) { asm volatile("v_mfma_f32_16x16x32_bf16 %0, %1, %2, %0" : "+v"(c) : "v"(a), "v"(b)); }
DI void mfma_init(f32x4& d, bf16x8 a, bf16x8 b) { asm volatile("v_mfma_f32_16x16x32_bf16 %0, %1, %2, 0" : "=&v"(d) : "v"(a), "v"(b)); }
#define MFMA_DRAIN() asm volatile("s_nop 7\n\ts_nop 1" ::: "memory")
DI float ex2(float x) { return __builtin_amdgcn_exp2f(x); }
template <bool NAT> DI void unit_voff(unsigned (&v)[4], int lane, unsigned stride) {
  const unsigned e = (lane >> 3) * stride + ((unsigned)((lane & 7) ^ (((lane >> 4) & (NAT ? 1 : 3)) << 1)) << 4);
#pragma unroll
  for (int p = 0; p < 4; ++p) v[p] = ((NAT && (p & 1)) ? e ^ 64 : e) + p * (8 * stride - 1024);
}
template <bool NAT> DI unsigned off_tr(int lane, int dt) {
  const int g = lane >> 4, qq = (lane >> 2) & 3, pp = lane & 3, f = ((qq >> 1) & 1) | ((g & 1) << 1);
  return ((NAT ? 8 : 4) * g + qq) * 128 + ((dt ^ f) << 5) + pp * 8;
}
template <bool NAT> DI bf16x8 ldfrag(unsigned a) { return __builtin_shufflevector(ldstr(a), ldstr(a + (NAT ? 512 : 2048)), 0, 1, 2, 3, 4, 5, 6, 7); }
DI unsigned off_x(int lane) { return (lane & 15) * 128 + (((lane >> 4) ^ (((lane & 15) >> 1) & 7)) << 4); }
DI bf16x8 ldp(unsigned a) {
  const bf16x4 lo = *(LAS bf16x4*)a, hi = *(LAS bf16x4*)(a + 512);
  return __builtin_shufflevector(lo, hi, 0, 1, 2, 3, 4, 5, 6, 7);
}
DI float xmax4(float m) {
  auto a = __builtin_amdgcn_permlane16_swap(__float_as_uint(m), __float_as_uint(m), false, false);
  m = fmaxf(__uint_as_float(a[0]), __uint_as_float(a[1]));
  auto c = __builtin_amdgcn_permlane32_swap(__float_as_uint(m), __float_as_uint(m), false, false);
  return fmaxf(__uint_as_float(c[0]), __uint_as_float(c[1]));
}
DI float xsum4(float m) {
  auto a = __builtin_amdgcn_permlane16_swap(__float_as_uint(m), __float_as_uint(m), false, false);
  m = __uint_as_float(a[0]) + __uint_as_float(a[1]);
  auto c = __builtin_amdgcn_permlane32_swap(__float_as_uint(m), __float_as_uint(m), false, false);
  return __uint_as_float(c[0]) + __uint_as_float(c[1]);
}

template <int IMM> DI void dma0(unsigned ldsaddr, unsigned voff, rsrc_t rs, unsigned soff) {
  asm volatile("s_mov_b32 m0, %0\n\ts_nop 0\n\tbuffer_load_dwordx4 %1, %2, %3 offen offset:%4 lds" ::"s"(ldsaddr), "v"(voff), "s"(rs), "s"(soff), "i"(IMM) : "memory");
}
template <int IMM> DI void dmaN(unsigned voff, rsrc_t rs, unsigned soff) {
  asm volatile("buffer_load_dwordx4 %0, %1, %2 offen offset:%3 lds" ::"v"(voff), "s"(rs), "s"(soff), "i"(IMM) : "memory");
}
DI void spacer(unsigned& dummy, rsrc_t rz) { asm volatile("buffer_load_dword %0, off, %1, 0" : "+{v255}"(dummy) : "s"(rz) : "memory"); }
DI void dma_unit(unsigned ldsaddr, const unsigned (&v)[4], rsrc_t rs, unsigned soff) {
  dma0<0>(ldsaddr, v[0], rs, soff); dmaN<1024>(v[1], rs, soff); dmaN<2048>(v[2], rs, soff); dmaN<3072>(v[3], rs, soff);
}
template <int NT, class MID>
DI void oproj(const bf16x8 (&af)[8][NT], const unsigned (&ob)[4], rsrc_t rsS, unsigned sbase, unsigned shalf, MID mid) {
  bf16x8 bf[2][8];
  f32x4 acc[2][NT];
  u32x2 keep = {0u, 0u};
#pragma unroll
  for (int j = 0; j < 8; ++j) bf[0][j] = lds128(ob[j >> 1] + (j & 1) * 5120);
  SBAR();
#pragma unroll
  for (int rt = 0; rt <= 5; ++rt) {
    const int c = rt & 1, p = c ^ 1;
    u32x4 d;
    if (rt == 5) MFMA_DRAIN();
#pragma unroll
    for (int i = 0; i < 8 * NT; ++i) {
      const int j = i / NT, n = i % NT;
      if (rt < 5) { if (j == 0) mfma_init(acc[c][n], af[j][n], bf[c][j]); else mfma_acc(acc[c][n], af[j][n], bf[c][j]); }
      if (rt < 4 && n == 1) bf[p][j] = lds128(ob[j >> 1] + (j & 1) * 5120 + (rt + 1) * 1024);
      if (rt > 0) {
        const int r = rt - 1;
        if (i == 8) { d[0] = cvtpk(acc[p][0][0], acc[p][0][1]); d[1] = cvtpk(acc[p][0][2], acc[p][0][3]); }
        if (i == 9) { d[2] = cvtpk(acc[p][1][0], acc[p][1][1]); d[3] = cvtpk(acc[p][1][2], acc[p][1][3]); }
        if (i == 11) __builtin_amdgcn_raw_buffer_store_b128(d, rsS, sbase + (NT == 4 ? r * 2048 : r * 1024), 0, 16);
        if (NT == 4) {
          if (i == 14) { d[0] = cvtpk(acc[p][2][0], acc[p][2][1]); d[1] = cvtpk(acc[p][2][2], acc[p][2][3]); }
          if (i == 15) { d[2] = cvtpk(acc[p][NT - 1][0], acc[p][NT - 1][1]); d[3] = cvtpk(acc[p][NT - 1][2], acc[p][NT - 1][3]); }
          if (i == 18) __builtin_amdgcn_raw_buffer_store_b128(d, rsS, sbase + r * 2048 + 1024, 0, 16);
        } else {
          if (i == 14) {
            const u32x2 e = {cvtpk(acc[p][2][0], acc[p][2][1]), cvtpk(acc[p][2][2], acc[p][2][3])};
            if (r & 1) d = u32x4{keep[0], keep[1], e[0], e[1]}; else keep = e;
          }
          if (i == 17 && (r & 1)) __builtin_amdgcn_raw_buffer_store_b128(d, rsS, sbase + (5 + (r >> 1)) * 1024, 0, 16);
          if (i == 17 && r == 4) __builtin_amdgcn_raw_buffer_store_b64(keep, rsS, shalf, 0, 16);
        }
      }
      SBAR();
    }
    if (rt == 0) mid();
  }
}
DI void zero_acc(f32x4 (&acc)[4][5]) {
#pragma unroll
  for (int dt = 0; dt < 4; ++dt)
#pragma unroll
    for (int rt = 0; rt < 5; ++rt) acc[dt][rt] = f32x4{0.f, 0.f, 0.f, 0.f};
}
DI float read_D(int rt, unsigned fl, int row) {
  float d[4];
#pragma unroll
  for (int w = 0; w < 4; ++w) {
    const float v = ldsf(L_DLT + (w * 80 + rt * 16 + row) * 4);
    d[w] = ((fl >> (8 * w)) & 1) ? v : 0.f;
  }
  return fmaxf(fmaxf(d[0], d[1]), fmaxf(d[2], d[3]));
}
DI float fadd_s(float a, float b) { float r = a + b; asm("" : "+v"(r)); return r; }
DI unsigned ls_addr(int w, int rt, int row) { return (rt < 4 ? L_K0 + rt * 64 : L_K1) + w * KSLOT + 8192 + row * 4; }

constexpr unsigned long long mkord(const int (&o)[16]) { unsigned long long r = 0; for (int i = 0; i < 16; ++i) r |= (unsigned long long)o[i] << (4 * i); return r; }
constexpr int ORD0[16] = {0, 4, 1, 2, 3, 8, 5, 6, 7, 12, 9, 10, 11, 13, 14, 15};
constexpr int ORD1[16] = {1, 0, 5, 2, 3, 4, 9, 6, 7, 8, 13, 10, 11, 12, 14, 15};
constexpr int ORD2[16] = {2, 0, 1, 6, 3, 4, 5, 10, 7, 8, 9, 14, 11, 12, 13, 15};
constexpr int ORD3[16] = {3, 0, 1, 2, 7, 4, 5, 6, 11, 8, 9, 10, 15, 12, 13, 14};
#define INL __attribute__((always_inline))

__global__ __launch_bounds__(512, 2) void gqa_fused(const void* __restrict__ x, const void* __restrict__ kc, const void* __restrict__ vc,
                                                    const void* __restrict__ wq, const void* __restrict__ wo, char* ws, char* __restrict__ out) {
  STAMP(0);
  TA(0);
  const int wid = __builtin_amdgcn_readfirstlane(threadIdx.x >> 6);
  if (wid >= 4) {
    unsigned long long pc;
    asm volatile("s_getpc_b64 %0" : "=s"(pc));
    const rsrc_t rsC = __builtin_amdgcn_make_buffer_rsrc((void*)(pc & ~127ull), 0, CODE_BYTES, 0x00020000);
    asm volatile("s_mov_b32 m0, 0x8000\n\ts_nop 4\n\tbuffer_load_dword %0, %1, %2 offen lds" ::"v"((threadIdx.x & 63) * 128), "s"(rsC), "s"((wid - 4) * 8192) : "memory");
  }
  SBAR();
  const int bid = blockIdx.x, kvh = bid & 7, q = (bid >> 3) & 3, b = bid >> 5, hg = q >> 1, ph = q & 1, h = 2 * kvh + hg;
  const int w = wid & 3, hf = wid >> 2;
  const unsigned ks0 = L_K0 + w * KSLOT, ks1 = L_K1 + w * KSLOT, vra = L_VR + w * 16384, vrb = vra + 8192;
  const unsigned long long ord = q == 0 ? mkord(ORD0) : q == 1 ? mkord(ORD1) : q == 2 ? mkord(ORD2) : mkord(ORD3);
#define TILE(t) ((unsigned)(ord >> (4 * (t))) & 15u)
  const rsrc_t rsZ = __builtin_amdgcn_make_buffer_rsrc((char*)wo, 0, 0, 0x00020000);
  const rsrc_t rsK = __builtin_amdgcn_make_buffer_rsrc((char*)kc + (size_t)(b * HKV + kvh) * SK * KVROW - 128, 0, ABL_KSZ(SK * KVROW + 128), 0x00020000);
  const rsrc_t rsV = __builtin_amdgcn_make_buffer_rsrc((char*)vc + (size_t)(b * HKV + kvh) * SK * KVROW, 0, ABL_VSZ(SK * KVROW), 0x00020000);
  unsigned dummy = 0;
#define SP() spacer(dummy, rsZ)
#define DMAK(P, slot, so)                                                                        \
  do {                                                                                           \
    if (((P) & 3) == 0) dma0<0>((slot) + ((P) >> 2) * 4224, kv0, rsK, (so) + ((P) >> 2) * 4096); \
    else dmaN<((P) & 3) * 1056>(kv0, rsK, (so) + ((P) >> 2) * 4096 - ((P) & 3) * 32);            \
  } while (0)

  bf16x8 kf[8];
  unsigned kv0, kra;
  {
    const rsrc_t rsX = __builtin_amdgcn_make_buffer_rsrc((char*)x + (size_t)(b * SQ + ph * 80) * XROW, 0, 80 * XROW, 0x00020000);
    const rsrc_t rsWQ = __builtin_amdgcn_make_buffer_rsrc((char*)wq, 0, DM * WQROW, 0x00020000);
    const int lane = lane_now();
    unsigned wv[4];
    unit_voff<true>(wv, lane, WQROW);
    const unsigned xv = (lane >> 3) * XROW + ((unsigned)((lane & 7) ^ (lane >> 4) ^ ((wid & 1) << 2)) << 4);
    const unsigned rot = (unsigned)(2 * b + ph) % 7u;
    auto chunk = [&](int s) INL { const unsigned c = rot + s; return c >= 7 ? c - 7 : c; };
    constexpr unsigned XS[7] = {L_K0, L_K0 + 10240, L_K0 + 20480, L_P0, L_P1, L_MS, L_P0};
    auto aslot = [&](int s) INL { return s % 3 == 0 ? vrb : s % 3 == 1 ? vra : ks1; };
    const unsigned wso = hf * 32 * WQROW + h * 512 + w * 128;
    auto req_x = [&](int s) INL {
      dma0<0>(XS[s] + wid * 1024, xv, rsX, wid * 8 * XROW + chunk(s) * 128);
      if (wid < 2) dma0<0>(XS[s] + (wid + 8) * 1024, xv, rsX, (wid + 8) * 8 * XROW + chunk(s) * 128);
    };
    TA(10);
    if (TOUCH == 2 && wid == 3) {
      const rsrc_t rsW = __builtin_amdgcn_make_buffer_rsrc(ws, 0, WS_SLAB + 256 * SLAB, 0x00020000);
      const int l = lane_now(), grp = b * 2 + ph;
      const unsigned o = l < 8 ? WS_FLAG + (grp * 8 + l) * 128 : l < 16 ? WS_EPOCH + (grp * 8 + l - 8) * 128 : l < 32 ? WS_SLAB + (grp * 16 + l - 16) * SLAB : 0x80000000u;
      asm volatile("buffer_load_dword %0, %1, %2, 0 offen sc1" : "+{v255}"(dummy) : "v"(o), "s"(rsW) : "memory");
    }
    req_x(0); dma_unit(aslot(0) + hf * 4096, wv, rsWQ, chunk(0) * 64 * WQROW + wso);
    asm volatile("s_barrier" ::: "memory");
    req_x(1); dma_unit(aslot(1) + hf * 4096, wv, rsWQ, chunk(1) * 64 * WQROW + wso);
    TA(1);
    kv0 = (lane >> 5) * 512 + (((lane & 31) ^ (lane >> 5)) << 4);
    kra = ks0 + ((lane & 15) >> 1) * 1056 + (lane & 1) * 512 + (((lane >> 4) ^ (lane & 1)) << 4);
    unsigned tv[2], tk[2], xk[2], xp[2];
#pragma unroll
    for (int i = 0; i < 2; ++i) {
      const unsigned o = off_tr<true>(lane, 2 * hf + i);
      tv[i] = vra + o; tk[i] = ks1 + o; xk[i] = off_x(lane) ^ (64 * i); xp[i] = L_P0 + xk[i];
      asm volatile("" : "+v"(tv[i]), "+v"(tk[i]), "+v"(xk[i]), "+v"(xp[i]));
    }
    f32x4 acc[2][5];
    bf16x8 a0[2], b0[5], a1[2], b1[5];
    auto stepA = [&](int mma, int RS, int RH, int DS, int DH, bf16x8 (&ca)[2], bf16x8 (&cb)[5], bf16x8 (&na)[2], bf16x8 (&nb)[5]) INL {
#pragma unroll
      for (int i = 0; i < 10; ++i) {
        if (mma == 1) mfma_acc(acc[i & 1][i >> 1], ca[i & 1], cb[i >> 1]);
        if (mma == 2) mfma_init(acc[i & 1][i >> 1], ca[i & 1], cb[i >> 1]);
        if (RS >= 0) {
          const unsigned xa = RS < 3 ? xk[RH] + XS[RS] : xp[RH] + (XS[RS] - L_P0);
          if (i == 0 || i == 2) na[i >> 1] = ldfrag<true>((RS % 3 == 2 ? tk[i >> 1] : tv[i >> 1] + (RS % 3 == 0 ? 8192 : 0)) + RH * 4096);
          if (i == 1) nb[0] = lds128(xa);
          if (i >= 3 && i <= 6) nb[i - 2] = lds128(xa + (i - 2) * 2048);
        }
        if (DS >= 0) {
          const unsigned so = chunk(DS) * 64 * WQROW + wso;
          if (i == 7) { if (DH == 0) dma0<0>(aslot(DS) + hf * 4096, wv[0], rsWQ, so); else dmaN<2048>(wv[2], rsWQ, so); }
          if (i == 9) { if (DH == 0) dmaN<1024>(wv[1], rsWQ, so); else dmaN<3072>(wv[3], rsWQ, so); }
        }
        SBAR();
      }
    };
    if (wid < 2) WAITVM(6); else WAITVM(5);
    BARRIER();
    TA(2);
    STAMP(1);
    TB(0);
    stepA(false, 0, 0, -1, 0, a1, b1, a0, b0);
    {
#pragma unroll
      for (int i = 0; i < 5; ++i) {
        const unsigned p = wid + 8 * i, cs = p / 10, j = p - cs * 10;
        const unsigned sl = cs == 0 ? XS[2] : L_P0 + (cs - 1) * 10240;
        unsigned c = rot + 2 + cs; c = c >= 7 ? c - 7 : c;
        dma0<0>(sl + j * 1024, xv, rsX, j * 8 * XROW + c * 128);
      }
      const unsigned so = chunk(2) * 64 * WQROW + wso;
      dma0<0>(aslot(2) + hf * 4096, wv[0], rsWQ, so); dmaN<1024>(wv[1], rsWQ, so);
      SBAR();
    }
    TB(1);
    stepA(2, 0, 1, 2, 1, a0, b0, a1, b1);
    TB(2);
    WAITVM(9);
    BARRIER();
    TB(3);
#pragma unroll
    for (int s = 1; s < 5; ++s) {
      if (s == 4) { req_x(6); SBAR(); }
      stepA(true, s, 0, s + 2, 0, a1, b1, a0, b0);
      if (s == 3) TB(10);
      stepA(true, s, 1, s + 2, 1, a0, b0, a1, b1);
      if (s == 1) TB(4);
      if (s == 2) TB(7);
      if (s == 3) TB(11);
      WAITVM(4);
      if (s == 1) TB(5);
      if (s == 2) TB(8);
      if (s == 3) TB(12);
      BARRIER();
      if (s == 1) TB(6);
      if (s == 2) TB(9);
      if (s == 3) TB(13);
      if (s == 4) TB(14);
    }
    TA(3);
    if (hf == 0) {
      const unsigned so = TILE(0) * 32768 + w * 8192 + 128;
      DMAK(0, ks0, so); DMAK(1, ks0, so); DMAK(2, ks0, so); DMAK(3, ks0, so);
      DMAK(4, ks0, so); DMAK(5, ks0, so); DMAK(6, ks0, so); DMAK(7, ks0, so);
    } else {
      unit_voff<false>(wv, lane, KVROW);
      dma_unit(vra, wv, rsV, TILE(0) * 32768 + w * 128);
      dma_unit(vra + 4096, wv, rsV, TILE(0) * 32768 + 16384 + w * 128);
    }
    SBAR();
    stepA(true, 5, 0, -1, 0, a1, b1, a0, b0);
    stepA(true, 5, 1, -1, 0, a0, b0, a1, b1);
    WAITVM(8);
    BARRIER();
    stepA(true, 6, 0, -1, 0, a1, b1, a0, b0);
    stepA(true, 6, 1, -1, 0, a0, b0, a1, b1);
    if (hf == 0) {
      WAITVM(0);
#pragma unroll
      for (int ks = 0; ks < 8; ++ks) kf[ks] = lds128(kra + ks * 64);
      SBAR();
    }
    stepA(true, -1, 0, -1, 0, a1, b1, a0, b0);
    TA(4);
    MFMA_DRAIN();
    const int ksq = 2 * w + hf, g = lane >> 4;
    const unsigned qa = (ksq < 6 ? L_K1 + ksq * 5120 : L_P1 + (ksq - 6) * 5120) + ((g >> 1) * 16 + (lane & 15)) * 16 + (g & 1) * 8;
#pragma unroll
    for (int rt = 0; rt < 5; ++rt)
#pragma unroll
      for (int u = 0; u < 2; ++u) {
        const f32x4 v = acc[u][rt] * QSCALE;
        *(LAS u32x2*)(qa + rt * 1024 + u * 512) = u32x2{cvtpk(v[0], v[1]), cvtpk(v[2], v[3])};
        SBAR();
      }
    if (wid == 4 && lane < 32) *(LAS unsigned*)(L_FLAG + lane * 4) = 0u;
    WAITLGKM0();
    TA(11);
    if (hf == 0) {
      const unsigned so = TILE(1) * 32768 + w * 8192 + 128;
      DMAK(0, ks0, so); DMAK(1, ks0, so); SP(); DMAK(2, ks0, so); DMAK(3, ks0, so); SP();
      DMAK(4, ks0, so); DMAK(5, ks0, so); SP(); DMAK(6, ks0, so); DMAK(7, ks0, so); SP();
    }
    TA(5);
    asm volatile("s_barrier" ::: "memory");
    TBSAVE(true);
    TA(6);
    STAMP(2);
  }

  const unsigned rotC = (unsigned)(2 * b + ph) & 3u;
  const rsrc_t rsWO = __builtin_amdgcn_make_buffer_rsrc((char*)wo + (size_t)h * DH * WOROW, 0, wid == 7 ? 0 : DH * WOROW, 0x00020000);
  unsigned long long epoch;
  gu64* const epw = (gu64*)(ws + WS_EPOCH + ((b * 2 + ph) * 8 + wid) * 128);
  const rsrc_t rsS = __builtin_amdgcn_make_buffer_rsrc(ws + WS_SLAB + (size_t)((b * 2 + ph) * 16 + h) * SLAB, 0, SLAB, 0x00020000);
  auto o_bases = [&](unsigned (&ob)[4], int lane) INL {
#pragma unroll
    for (int i = 0; i < 4; ++i) { const unsigned cc = (rotC + i) & 3; ob[i] = (cc == 3 ? L_OI3 : L_P0 + cc * 10240) + lane * 16; }
  };

  if (wid < 4) {
    unsigned pwa;
    bf16x8 qf[5][8];
    {
      const int lane = lane_now();
      pwa = L_P0 + (w >> 1) * 5120 + (PLAY == 0 ? (w & 1) * 512 + lane * 8 : lane * 16 + (w & 1) * 8);
      unsigned q0 = L_K1 + lane * 16, q1 = L_P1 + lane * 16;
      asm volatile("" : "+v"(q0), "+v"(q1));
#pragma unroll
      for (int rt = 0; rt < 5; ++rt)
#pragma unroll
        for (int ks = 0; ks < 8; ++ks) qf[rt][ks] = lds128((ks < 6 ? q0 + ks * 5120 : q1 + (ks - 6) * 5120) + rt * 1024);
      SBAR();
    }
    float lsum[5] = {0.f, 0.f, 0.f, 0.f, 0.f}, tsr[5] = {0.f, 0.f, 0.f, 0.f, 0.f};
    f32x4 S0 = {0.f, 0.f, 0.f, 0.f};
#pragma unroll
    for (int ks = 0; ks < 8; ++ks) S0 = mfma(kf[ks], qf[0][ks], S0);
    TA(7);
    unsigned dslot = ks1;
    const unsigned pwsum = 2 * pwa + 10240, krsum = 2 * kra + L_K1;
    auto qk_tile = [&](int t, auto wo_c) INL -> unsigned {
      constexpr bool WO = decltype(wo_c)::value;
      unsigned so, wv[4];
      if constexpr (WO) { unit_voff<false>(wv, lane_now(), WOROW); so = ((rotC + t - 14) & 3) * 64 * WOROW + w * 128; }
      else so = TILE(t + 2) * 32768 + w * 8192 + 128;
      unsigned fa = L_FLAG + t * 4;
      asm volatile("" : "+v"(fa));
      asm volatile("" : "+s"(so));
      TR(11);
      BARRIER();
      TR(12);
      TRSAVE(t == TRT + 1);
      if (t == 0) TA(8);
      if (t == 1) { TA(9); TASAVE(true); }
      unsigned fl;
      f32x4 S1;
      float p0, p1, p2, p3, tm;
      u32x2 pk;
#define QDMA(P)                                                                                                   \
  do {                                                                                                            \
    if constexpr (WO) {                                                                                           \
      if (((P) & 3) == 0) dma0<0>(dslot + ((P) >> 2) * 4096, wv[0], rsWO, so + ((P) >> 2) * 32 * WOROW);           \
      else dmaN<((P) & 3) * 1024>(wv[(P) & 3], rsWO, so + ((P) >> 2) * 32 * WOROW);                                \
    } else DMAK(P, dslot, so);                                                                                    \
  } while (0)
#define QM(i, RN, SN) if ((i) == 0) mfma_init(SN, kf[i], qf[RN][i]); else mfma_acc(SN, kf[i], qf[RN][i]);
#define QKBURST(RC, SC, RN, SN)                                                                                    \
  TR(2 * RC); QM(0, RN, SN) SBAR(); PRIO_HI(); p0 = ex2(SC[0]); SBAR();                                            \
  QM(1, RN, SN) SBAR(); p1 = ex2(SC[1]); SBAR();                                                                   \
  QM(2, RN, SN) SBAR(); p2 = ex2(SC[2]); SBAR();                                                                   \
  QM(3, RN, SN) SBAR(); p3 = ex2(SC[3]); SBAR();                                                                   \
  QM(4, RN, SN) SBAR(); pk[0] = cvtpk(p0, p1); p0 = fadd_s(p0, p1); SBAR();                                        \
  QM(5, RN, SN) SBAR(); pk[1] = cvtpk(p2, p3); p2 = fadd_s(p2, p3); SBAR();                                        \
  QM(6, RN, SN) SBAR(); lsum[RC] = fadd_s(lsum[RC], tsr[RC]); tsr[RC] = fadd_s(p0, p2); SBAR();                    \
  QM(7, RN, SN) SBAR(); PRIO_LO(); TR(2 * RC + 1);
#define QKFIN(RC)                                                                                                  \
  *(LAS u32x2*)(pwa + RC * 1024) = pk;                                                                             \
  tm = RC == 0 ? tsr[0] : fadd_s(tm, tsr[RC]); SBAR();
      QKBURST(0, S0, 1, S1)
      fl = *(LAS unsigned*)fa;
      QKFIN(0)
      QDMA(0); QDMA(1); SP(); SBAR();
      QKBURST(1, S1, 2, S0)
      QKFIN(1)
      unsigned long long bad = __ballot(fl != 0);
      QDMA(2); QDMA(3); SP(); SBAR();
      QKBURST(2, S0, 3, S1)
      QKFIN(2)
      QDMA(4); QDMA(5); SP(); SBAR();
      QKBURST(3, S1, 4, S0)
      WAITVM(10);
#pragma unroll
      for (int ks = 0; ks < 8; ++ks) kf[ks] = lds128(kra + ks * 64);
      kra = krsum - kra;
      asm volatile("" : "+v"(kra));
      SBAR();
      QKFIN(3)
      QKBURST(4, S0, 0, S1)
      QKFIN(4)
      bad |= __ballot(!(tm <= PTHR));
      pwa = pwsum - pwa;
      asm volatile("" : "+v"(pwa));
      SBAR();
      QDMA(6); QDMA(7); SP(); SBAR();
      dslot = ks0 + ks1 - dslot;
      S0 = S1;
#undef QKFIN
#undef QKBURST
#undef QM
#undef QDMA
      if (!WO && __builtin_expect(t == 0, 0)) bad |= __ballot(!(fminf(fminf(fminf(tsr[0], tsr[1]), fminf(tsr[2], tsr[3])), tsr[4]) >= PLOW));
      TR(10);
      return (unsigned)bad | (unsigned)(bad >> 32);
    };
    int t = -1;
    unsigned abn, last = 0x4000u;
#pragma unroll 1
    do { ++t; last >>= 1; asm volatile("" : "+s"(last)); abn = qk_tile(t, std::false_type{}); } while ((abn | (last & 1u)) == 0);
    if (abn == 0) {
#pragma unroll 1
      do { ++t; abn = qk_tile(t, std::true_type{}); } while ((abn | (t & 1)) == 0);
    }
    int entry = 2;
    if (abn == 0) {
#pragma unroll
      for (int rt = 0; rt < 5; ++rt) lsum[rt] += tsr[rt];
      t = 16;
      BARRIER();
      entry = __builtin_amdgcn_readfirstlane(*(LAS unsigned*)(L_FLAG + 64)) != 0;
    } else {
      kra = krsum - kra;
      pwa = pwsum - pwa;
    }
    if (__builtin_expect(entry != 0, 0)) {
      MFMA_DRAIN();
      float negm[5] = {0.f, 0.f, 0.f, 0.f, 0.f};
#pragma unroll 1
      for (;; ++t) {
        if (entry == 0) BARRIER();
        {
          const unsigned fl = __builtin_amdgcn_readfirstlane(*(LAS unsigned*)(L_FLAG + t * 4));
          if (fl != 0) {
            const unsigned pprev = pwsum - pwa;
            const int row = lane_now() & 15;
#pragma unroll
            for (int rt = 0; rt < 5; ++rt) {
              const float D = read_D(rt, fl, row);
              const float own = ((fl >> (8 * w)) & 1) ? ldsf(L_DLT + (w * 80 + rt * 16 + row) * 4) : 0.f;
              const float f = ex2(own - D);
              const u32x2 pk = *(LAS u32x2*)(pprev + rt * 1024);
              const float a0 = __uint_as_float(pk[0] << 16) * f, a1 = __uint_as_float(pk[0] & 0xffff0000u) * f;
              const float a2 = __uint_as_float(pk[1] << 16) * f, a3 = __uint_as_float(pk[1] & 0xffff0000u) * f;
              *(LAS u32x2*)(pprev + rt * 1024) = u32x2{cvtpk(a0, a1), cvtpk(a2, a3)};
              lsum[rt] *= f;
              negm[rt] -= D;
              SBAR();
            }
            BARRIER();
          }
        }
        if (t == 16) break;
        if (entry != 2) {
          const unsigned dslot = (t & 1) ? ks0 : ks1;
          if (t < 14) {
            const unsigned so = TILE(t + 2) * 32768 + w * 8192 + 128;
            DMAK(0, dslot, so); DMAK(1, dslot, so); DMAK(2, dslot, so); DMAK(3, dslot, so);
            DMAK(4, dslot, so); DMAK(5, dslot, so); DMAK(6, dslot, so); DMAK(7, dslot, so);
          } else {
            unsigned wv[4];
            unit_voff<false>(wv, lane_now(), WOROW);
            const unsigned so = ((rotC + t - 14) & 3) * 64 * WOROW + w * 128;
            dma_unit(dslot, wv, rsWO, so);
            dma_unit(dslot + 4096, wv, rsWO, so + 32 * WOROW);
          }
        } else {
          const int lane = lane_now();
#pragma unroll
          for (int ks = 0; ks < 8; ++ks)
            kf[ks] = __builtin_bit_cast(bf16x8, __builtin_amdgcn_raw_buffer_load_b128(rsK, (lane & 15) * 512 + (lane >> 4) * 16 + ks * 64, TILE(t) * 32768 + w * 8192 + 128, 0));
        }
        f32x4 S[5];
        float mx[5];
        bool need = false;
#pragma unroll
        for (int rt = 0; rt < 5; ++rt) {
          S[rt] = f32x4{negm[rt], negm[rt], negm[rt], negm[rt]};
#pragma unroll
          for (int ks = 0; ks < 8; ++ks) S[rt] = mfma(kf[ks], qf[rt][ks], S[rt]);
          mx[rt] = ceilf(xmax4(fmaxf(fmaxf(S[rt][0], S[rt][1]), fmaxf(S[rt][2], S[rt][3]))));
          need |= !(mx[rt] <= GTHR) || (t == 0 && !(mx[rt] >= -GTHR));
          SBAR();
        }
        need = __any(need);
        const int lane = lane_now();
#pragma unroll
        for (int rt = 0; rt < 5; ++rt) {
          const float dl = need ? (t > 0 ? fmaxf(mx[rt], 0.f) : mx[rt]) : 0.f;
          const float p0 = ex2(S[rt][0] - dl), p1 = ex2(S[rt][1] - dl), p2 = ex2(S[rt][2] - dl), p3 = ex2(S[rt][3] - dl);
          const float ts = (p0 + p1) + (p2 + p3);
          lsum[rt] = t > 0 ? lsum[rt] * ex2(-dl) + ts : ts;
          *(LAS u32x2*)(pwa + rt * 1024) = u32x2{cvtpk(p0, p1), cvtpk(p2, p3)};
          if (need && lane < 16) *(LAS float*)(L_DLT + (w * 80 + rt * 16 + lane) * 4) = dl;
          SBAR();
        }
        if (need) *(LAS unsigned char*)(L_FLAG + (t + 1) * 4 + w) = 1;
        WAITVM(8);
#pragma unroll
        for (int ks = 0; ks < 8; ++ks) kf[ks] = lds128(kra + ks * 64);
        kra = krsum - kra;
        pwa = pwsum - pwa;
        entry = 0;
      }
    }
    {
      const int lane = lane_now();
#pragma unroll
      for (int rt = 0; rt < 5; ++rt) {
        const float l = xsum4(lsum[rt]);
        if (lane < 16) *(LAS float*)ls_addr(w, rt, lane) = l;
      }
    }
    const int lane = lane_now();
    unsigned t1[4], wv[4], ob[4];
#pragma unroll
    for (int dt = 0; dt < 4; ++dt) t1[dt] = ks0 + off_tr<false>(lane, dt);
    unit_voff<false>(wv, lane, WOROW);
    o_bases(ob, lane);
    bf16x8 af[8][4];
    const unsigned so2 = ((rotC + 2) & 3) * 64 * WOROW, so3 = ((rotC + 3) & 3) * 64 * WOROW;
    WAITVM(8);
#pragma unroll
    for (int j = 0; j < 2; ++j)
#pragma unroll
      for (int dt = 0; dt < 4; ++dt) af[j][dt] = ldfrag<false>(t1[dt] + L_K1 + j * 4096);
    WAITLGKM0();
    dma_unit(ks1, wv, rsWO, so2 + w * 128); SP();
    dma_unit(ks1 + 4096, wv, rsWO, so2 + 32 * WOROW + w * 128); SP();
    WAITVM(10);
#pragma unroll
    for (int j = 2; j < 4; ++j)
#pragma unroll
      for (int dt = 0; dt < 4; ++dt) af[j][dt] = ldfrag<false>(t1[dt] + (j & 1) * 4096);
    BARRIER();
    STAMP(5);
    dma_unit(ks0, wv, rsWO, so3 + w * 128); SP();
    dma_unit(ks0 + 4096, wv, rsWO, so3 + 32 * WOROW + w * 128); SP();
#pragma unroll
    for (int j = 4; j < 8; ++j) {
      if (w < 3) WAITVM(16); else if (j == 4) WAITVM(16); else if (j == 5) WAITVM(11); else if (j == 6) WAITVM(6); else WAITVM(1);
#pragma unroll
      for (int dt = 0; dt < 4; ++dt) af[j][dt] = ldfrag<false>(t1[dt] + (j < 6 ? L_K1 : 0) + (j & 1) * 4096);
      WAITLGKM0();
      if (w < 3) { dma_unit((j < 6 ? ks1 : ks0) + (j & 1) * 4096, wv, rsWO, (j < 6 ? so2 : so3) + (j & 1) * 32 * WOROW + (4 + w) * 128); SP(); }
    }
    asm volatile("s_barrier" ::: "memory");
    STAMP(6);
    oproj<4>(af, ob, rsS, w * 10240 + lane * 16, 0, [&]() INL {
      WAITVM(0);
      asm volatile("s_barrier" ::: "memory");
      epoch = __hip_atomic_load(epw + (lane >> 6), __ATOMIC_RELAXED, __HIP_MEMORY_SCOPE_AGENT);
    });
    STAMP(7);
  } else {
    const int lane = lane_now(), row = lane & 15;
    unsigned ta[4], dv[4];
#pragma unroll
    for (int dt = 0; dt < 4; ++dt) ta[dt] = vra + off_tr<false>(lane, dt);
    unit_voff<false>(dv, lane, KVROW);
    if (TOUCH == 1 && wid == 7) {
      const rsrc_t rsW = __builtin_amdgcn_make_buffer_rsrc(ws, 0, WS_SLAB + 256 * SLAB, 0x00020000);
      const int l = lane_now(), grp = b * 2 + ph;
      const unsigned o = l < 8 ? WS_FLAG + (grp * 8 + l) * 128 : l < 16 ? WS_EPOCH + (grp * 8 + l - 8) * 128 : l < 32 ? WS_SLAB + (grp * 16 + l - 16) * SLAB : 0x80000000u;
      asm volatile("buffer_load_dword %0, %1, %2, 0 offen sc1" : "+{v255}"(dummy) : "v"(o), "s"(rsW) : "memory");
    }
    dma_unit(vrb, dv, rsV, TILE(1) * 32768 + w * 128);
    SP();
    dma_unit(vrb + 4096, dv, rsV, TILE(1) * 32768 + 16384 + w * 128);
    SP();
    f32x4 acc[4][5];
    zero_acc(acc);
    bf16x8 va[2][4], vb[2][4];
    WAITVM(10);
#pragma unroll
    for (int hh = 0; hh < 2; ++hh)
#pragma unroll
      for (int dt = 0; dt < 4; ++dt) va[hh][dt] = ldfrag<false>(ta[dt] + hh * 4096);
    unsigned pa = L_P0 + lane * (PLAY == 0 ? 8 : 16);
    asm volatile("" : "+v"(pa));
    TA(7);
    BARRIER();
    TA(8);
    TASAVE(true);
    rsrc_t rsD = rsV;
    unsigned dstep = 16384;
    auto pv_tile = [&](int t, auto par_c, bf16x8 (&vc)[2][4], bf16x8 (&vn)[2][4]) INL {
      constexpr int PAR = decltype(par_c)::value;
      unsigned so;
      if (t < 14) so = TILE(t + 2) * 32768 + w * 128;
      else so = ((rotC + t - 14) & 3) * 64 * WOROW + wid * 128;
      const unsigned dsl = vra + PAR * 8192;
      unsigned fa = L_FLAG + (t + 1) * 4;
      asm volatile("" : "+v"(fa));
      asm volatile("" : "+s"(so));
      bf16x8 pf[5][2];
#define RDP(rt) { pf[rt][0] = ldp(pa + PAR * 10240 + (rt) * 1024); pf[rt][1] = ldp(pa + PAR * 10240 + 5120 + (rt) * 1024); }
#define PM(i, rt) mfma_acc(acc[(i) & 3][rt], vc[(i) >> 2][(i) & 3], pf[rt][(i) >> 2]);
#define RDV(dt, hh) vn[hh][dt] = ldfrag<false>(ta[dt] + (PAR ^ 1) * 8192 + (hh) * 4096);
#define BURST(rt)                                                                                                  \
  SBAR(); TR(2 * rt + 1); PM(0, rt) SBAR(); PRIO_HI(); SBAR(); PM(1, rt) PM(2, rt) PM(3, rt) PM(4, rt) PM(5, rt) PM(6, rt) PM(7, rt) SBAR(); PRIO_LO(); TR(2 * rt + 2); SBAR();
#define PD(P)                                                                                                      \
  {                                                                                                                \
    if (((P) & 3) == 0) dma0<0>(dsl + ((P) >> 2) * 4096, dv[0], rsD, (P) ? so + dstep : so);                       \
    else dmaN<((P) & 3) * 1024>(dv[(P) & 3], rsD, (P) >> 2 ? so + dstep : so);                                     \
  }
      TR(11);
      BARRIER();
      TR(12);
      TRSAVE(t == TRT);
      TR(0);
      unsigned fl = *(LAS unsigned*)fa;
      RDP(0) RDP(1)
      SBAR();
      PD(0) SP();
      SBAR();
      fl = __builtin_amdgcn_readfirstlane(fl);
      if (__builtin_expect(fl != 0, 0)) {
        if (t > 0) {
#pragma unroll
          for (int rt = 0; rt < 5; ++rt) {
            const float f = ex2(-read_D(rt, fl, row));
#pragma unroll
            for (int dt = 0; dt < 4; ++dt) acc[dt][rt] *= f;
          }
        }
        BARRIER();
        RDP(0) RDP(1)
      }
      BURST(0)
      RDP(2) SBAR();
      PD(1) PD(2) SP();
      BURST(1)
      RDP(3) SBAR();
      WAITVM(11);
      RDV(0, 0) RDV(1, 0) SBAR();
      PD(3)
      BURST(2)
      RDP(4) RDV(2, 0) RDV(3, 0) SBAR();
      PD(4) PD(5) SP();
      if (PAR == 1 && t == 15) BARRIER();
      BURST(3)
      WAITVM(10);
      RDV(0, 1) RDV(1, 1) RDV(2, 1) RDV(3, 1) SBAR();
      PD(6) PD(7) SP();
      BURST(4)
#undef PD
#undef BURST
#undef RDV
#undef PM
#undef RDP
    };
#pragma unroll 1
    for (int t = 0; t < 16; t += 2) {
      if (t == 14) { unit_voff<false>(dv, lane_now(), WOROW); rsD = rsWO; dstep = 32 * WOROW; }
      pv_tile(t, std::integral_constant<int, 0>{}, va, vb);
      pv_tile(t + 1, std::integral_constant<int, 1>{}, vb, va);
    }
    STAMP(5);
    float inv[5];
#pragma unroll
    for (int rt = 0; rt < 5; ++rt) {
      float l = 0.f;
#pragma unroll
      for (int ww = 0; ww < 4; ++ww) l += ldsf(ls_addr(ww, rt, row));
      inv[rt] = __builtin_amdgcn_rcpf(l);
    }
    if (w == 3) WAITVM(0);
    MFMA_DRAIN();
    SBAR();
    const unsigned oa = (w == 3 ? L_OI3 : L_P0 + w * 10240) + lane * 16;
#pragma unroll
    for (int rt = 0; rt < 5; ++rt)
#pragma unroll
      for (int ks = 0; ks < 2; ++ks) {
        const f32x4 u = acc[2 * ks][rt] * inv[rt], v = acc[2 * ks + 1][rt] * inv[rt];
        *(LAS u32x4*)(oa + ks * 5120 + rt * 1024) = u32x4{cvtpk(u[0], u[1]), cvtpk(u[2], u[3]), cvtpk(v[0], v[1]), cvtpk(v[2], v[3])};
        SBAR();
      }
    unsigned ob[4], tA[3], tB[3];
    o_bases(ob, lane);
#pragma unroll
    for (int i = 0; i < 3; ++i) {
      const int T = 3 * w + i, o = T >> 2;
      const unsigned tr = off_tr<false>(lane, T & 3);
      tA[i] = L_VR + o * 16384 + tr; tB[i] = L_K0 + o * KSLOT + tr;
    }
    WAITVM(0);
    BARRIER();
    STAMP(6);
    bf16x8 af[8][3];
#pragma unroll
    for (int j = 0; j < 4; ++j)
#pragma unroll
      for (int i = 0; i < 3; ++i) af[j][i] = ldfrag<false>(tA[i] + j * 4096);
    asm volatile("s_barrier" ::: "memory");
#pragma unroll
    for (int j = 4; j < 8; ++j)
#pragma unroll
      for (int i = 0; i < 3; ++i) af[j][i] = ldfrag<false>(tB[i] + (j < 6 ? L_K1 : 0) + (j & 1) * 4096);
    oproj<3>(af, ob, rsS, 40960 + w * 7680 + lane * 16, 40960 + w * 7680 + 7168 + lane * 8, [&]() INL { epoch = __hip_atomic_load(epw + (lane >> 6), __ATOMIC_RELAXED, __HIP_MEMORY_SCOPE_AGENT); });
    STAMP(7);
  }
  {
    unsigned long long tag = epoch + 0x9E3779B97F4A7C15ull;
    tag = (tag ^ (tag >> 30)) * 0xBF58476D1CE4E5B9ull;
    tag = (tag ^ (tag >> 27)) * 0x94D049BB133111EBull;
    tag ^= tag >> 31;
    const int lane = lane_now();
    gu64* fl = (gu64*)(ws + WS_FLAG + ((b * 2 + ph) * 8 + wid) * 128);
    const int nch = hf ? 30 : 40, r = h * nch + (lane < nch ? lane : 0), piece = r >> 6;
    int nt0, nt1, rt0, rt1, l0 = r & 63, l1 = r & 63;
    if (hf == 0) {
      nt0 = 4 * w + 2 * (piece & 1); nt1 = nt0 + 1; rt0 = rt1 = piece >> 1;
    } else {
      nt0 = 16 + 3 * w + (piece < 5 ? 0 : 2); nt1 = 16 + 3 * w + (piece < 5 ? 1 : 2);
      rt0 = piece < 5 ? piece : 2 * (piece - 5); rt1 = piece < 5 ? piece : piece < 7 ? 2 * (piece - 5) + 1 : 4;
      if (piece == 7) { l0 = 2 * l0; l1 = l0 + 1; }
    }
    const unsigned d0 = ((16 * rt0 + (l0 & 15)) * DM + 16 * nt0 + 4 * (l0 >> 4)) * 2, d1 = ((16 * rt1 + (l1 & 15)) * DM + 16 * nt1 + 4 * (l1 >> 4)) * 2;
    const unsigned src = lane < nch ? (hf ? 40960 + w * 7680 : w * 10240) + r * 16 : 0x80000000u;
    const rsrc_t rsG = __builtin_amdgcn_make_buffer_rsrc(ws + WS_SLAB + (size_t)(b * 2 + ph) * 16 * SLAB, 0, 16 * SLAB, 0x00020000);
    unsigned long long f = ~tag;
    if (hf == 0) f = __hip_atomic_load(fl + (lane & 15), __ATOMIC_RELAXED, __HIP_MEMORY_SCOPE_AGENT);
    WAITVM(0);
    asm volatile("" :: "{v255}"(dummy));
    STAMP(12);
    if (lane == 0) __hip_atomic_store(fl + h, tag, __ATOMIC_RELAXED, __HIP_MEMORY_SCOPE_AGENT);
    u32x4 v[16];
#pragma unroll
    for (int hh = 0; hh < 16; ++hh) v[hh] = u32x4{0u, 0u, 0u, 0u};
    unsigned pend = 0xffffu;
#pragma unroll 1
    for (unsigned spin = 0; spin < (1u << 22); ++spin) {
      const unsigned rdy = ((unsigned)__ballot(f == tag) | (1u << h)) & pend;
      __builtin_amdgcn_fence(__ATOMIC_ACQUIRE, "wavefront");
      asm volatile("" ::: "memory");
#pragma unroll
      for (int hh = 0; hh < 16; ++hh)
        if ((rdy >> hh) & 1) v[hh] = __builtin_amdgcn_raw_buffer_load_b128(rsG, src, hh * SLAB, 16);
      pend &= ~rdy;
      if (pend == 0) break;
      if (rdy == 0) __builtin_amdgcn_s_sleep(SLP);
      f = __hip_atomic_load(fl + (lane & 15), __ATOMIC_RELAXED, __HIP_MEMORY_SCOPE_AGENT);
    }
    STAMP(13);
    if (lane < nch) {
      if (h == 0 && lane == 0) __hip_atomic_store(epw, epoch + 1, __ATOMIC_RELAXED, __HIP_MEMORY_SCOPE_AGENT);
      float sm[8] = {0.f, 0.f, 0.f, 0.f, 0.f, 0.f, 0.f, 0.f};
#pragma unroll
      for (int hh = 0; hh < 16; ++hh)
#pragma unroll
        for (int i = 0; i < 4; ++i) { sm[2 * i] += __uint_as_float(v[hh][i] << 16); sm[2 * i + 1] += __uint_as_float(v[hh][i] & 0xffff0000u); }
      char* dst = out + (size_t)(b * 2 + ph) * 80 * DM * 2;
      *(u32x2*)(dst + d0) = u32x2{cvtpk(sm[0], sm[1]), cvtpk(sm[2], sm[3])};
      *(u32x2*)(dst + d1) = u32x2{cvtpk(sm[4], sm[5]), cvtpk(sm[6], sm[7])};
    }
  }
}

extern "C" void kernel_launch(void* const* d_in, const int* in_sizes, int n_in, void* d_out, int out_size, void* d_ws, size_t ws_size,
                              hipStream_t stream) {
  (void)in_sizes; (void)n_in; (void)out_size; (void)ws_size;
  (void)hipFuncSetAttribute((const void*)gqa_fused, hipFuncAttributeMaxDynamicSharedMemorySize, L_TOTAL);
  gqa_fused<<<256, 512, L_TOTAL, stream>>>(d_in[0], d_in[1], d_in[2], d_in[3], d_in[4], (char*)d_ws, (char*)d_out);
}
```

```cpp
#define STAMPS
#define DIAG 0
#include <hip/hip_runtime.h>
#include <hip/hip_bf16.h>
#include <type_traits>
#define PP 1
#define SPT 1
#define SPCQ 1
#define SPCP 1
#define TRT 7
#define SLP 4
#define TOUCH 2
#define PLAY 0
#define CPF 1
#define ABL 0
#pragma clang diagnostic ignored "-Wint-to-pointer-cast"

typedef __bf16 bf16x8 __attribute__((ext_vector_type(8)));
typedef __bf16 bf16x4 __attribute__((ext_vector_type(4)));
typedef __bf16 bf16x2 __attribute__((ext_vector_type(2)));
typedef float f32x4 __attribute__((ext_vector_type(4)));
typedef float f32x2 __attribute__((ext_vector_type(2)));
typedef unsigned u32x4 __attribute__((ext_vector_type(4)));
typedef unsigned u32x2 __attribute__((ext_vector_type(2)));
#define LAS __attribute__((address_space(3)))
#define DI __device__ __forceinline__
typedef __amdgpu_buffer_rsrc_t rsrc_t;

constexpr int NB = 8, SQ = 160, DM = 448, HQ = 16, HKV = 8, SK = 1024, DH = 256;
constexpr unsigned XROW = DM * 2, WQROW = HQ * DH * 2, KVROW = DH * 2, WOROW = DM * 2;
constexpr unsigned SLAB = 80 * DM * 2;
constexpr unsigned WS_FLAG = 0, WS_EPOCH = 32768, WS_SLAB = 65536;
typedef __attribute__((address_space(1))) unsigned long long gu64;
constexpr unsigned KSLOT = 8448;
constexpr unsigned L_K0 = 0, L_K1 = 4 * KSLOT, L_VR = 8 * KSLOT, L_P0 = L_VR + 65536, L_P1 = L_P0 + 10240, L_MS = L_P1 + 10240, L_TOTAL = L_MS + 10240;
static_assert(L_TOTAL == 163840, "LDS");
constexpr unsigned L_FLAG = L_MS;
constexpr unsigned L_DLT = L_MS + 128;
constexpr unsigned L_OI3 = L_VR + 3 * 16384;
constexpr int CODE_BYTES = 26624;
constexpr float QSCALE = 0.0625f * 1.4426950408889634f;
#define PTHR 65536.f
#define GTHR 12.f
#define PLOW (1.f / 65536.f)

#define NSTAMP 14
#define STAMP_OFF (64u << 20)
#define STAMP(k) do { __builtin_amdgcn_sched_barrier(0); asm volatile("s_memrealtime %0\n\ts_waitcnt lgkmcnt(0)" : "=s"(stamp[k]) :: "memory"); __builtin_amdgcn_sched_barrier(0); } while (0)
#define ABL_KSZ(x) ((ABL & 1) ? 0 : (x))
#define ABL_VSZ(x) ((ABL & 1024) ? 0 : (x))
#define SBAR() __builtin_amdgcn_sched_barrier(0)
#define TRX(k) do { SBAR(); asm volatile("s_memtime %0" : "=s"(tr[k])); SBAR(); } while (0)
#define TRXSAVE(cond) do { if (cond) { WAITLGKM0(); if (lane_now() == 0) for (int k_ = 0; k_ < 16; ++k_) ((unsigned long long*)(ws + (80u << 20)))[(bid * 8 + wid) * 16 + k_] = tr[k_]; } } while (0)
#define TR(k)
#define TRSAVE(cond)
#define TB(k)
#define TBSAVE(cond)
#define TA(k)
#define TASAVE(cond)
#define PRIO_HI() __builtin_amdgcn_s_setprio(1)
#define PRIO_LO() __builtin_amdgcn_s_setprio(0)
#define WAITVM(n) asm volatile("s_waitcnt vmcnt(%0)" ::"i"(n) : "memory")
#define WAITLGKM0() do { asm volatile("" ::: "memory"); __builtin_amdgcn_s_waitcnt(0xC07F); asm volatile("" ::: "memory"); } while (0)
#define BARRIER() do { WAITLGKM0(); asm volatile("s_barrier" ::: "memory"); } while (0)

DI int lane_now() { unsigned z = 0; asm volatile("" : "+v"(z)); return __builtin_amdgcn_mbcnt_hi(~0u, __builtin_amdgcn_mbcnt_lo(~0u, z)); }
DI bf16x8 lds128(unsigned a) { return *(LAS bf16x8*)a; }
DI bf16x4 ldstr(unsigned a) { return __builtin_amdgcn_ds_read_tr16_b64_v4bf16((LAS bf16x4*)a); }
DI float ldsf(unsigned a) { return *(LAS float*)a; }
DI unsigned cvtpk(float lo, float hi) { f32x2 v = {lo, hi}; return __builtin_bit_cast(unsigned, __builtin_convertvector(v, bf16x2)); }
DI f32x4 mfma(bf16x8 a, bf16x8 b, f32x4 c) { return __builtin_amdgcn_mfma_f32_16x16x32_bf16(a, b, c, 0, 0, 0); }
DI void mfma_acc(f32x4& c, bf16x8 a, bf16x8 b) { asm volatile("v_mfma_f32_16x16x32_bf16 %0, %1, %2, %0" : "+v"(c) : "v"(a), "v"(b)); }
DI void mfma_init(f32x4& d, bf16x8 a, bf16x8 b) { asm volatile("v_mfma_f32_16x16x32_bf16 %0, %1, %2, 0" : "=&v"(d) : "v"(a), "v"(b)); }
#define MFMA_DRAIN() asm volatile("s_nop 7\n\ts_nop 1" ::: "memory")
DI float ex2(float x) { return __builtin_amdgcn_exp2f(x); }
template <bool NAT> DI void unit_voff(unsigned (&v)[4], int lane, unsigned stride) {
  const unsigned e = (lane >> 3) * stride + ((unsigned)((lane & 7) ^ (((lane >> 4) & (NAT ? 1 : 3)) << 1)) << 4);
#pragma unroll
  for (int p = 0; p < 4; ++p) v[p] = ((NAT && (p & 1)) ? e ^ 64 : e) + p * (8 * stride - 1024);
}
template <bool NAT> DI unsigned off_tr(int lane, int dt) {
  const int g = lane >> 4, qq = (lane >> 2) & 3, pp = lane & 3, f = ((qq >> 1) & 1) | ((g & 1) << 1);
  return ((NAT ? 8 : 4) * g + qq) * 128 + ((dt ^ f) << 5) + pp * 8;
}
template <bool NAT> DI bf16x8 ldfrag(unsigned a) { return __builtin_shufflevector(ldstr(a), ldstr(a + (NAT ? 512 : 2048)), 0, 1, 2, 3, 4, 5, 6, 7); }
DI unsigned off_x(int lane) { return (lane & 15) * 128 + (((lane >> 4) ^ (((lane & 15) >> 1) & 7)) << 4); }
DI bf16x8 ldp(unsigned a) {
  const bf16x4 lo = *(LAS bf16x4*)a, hi = *(LAS bf16x4*)(a + 512);
  return __builtin_shufflevector(lo, hi, 0, 1, 2, 3, 4, 5, 6, 7);
}
DI float xmax4(float m) {
  auto a = __builtin_amdgcn_permlane16_swap(__float_as_uint(m), __float_as_uint(m), false, false);
  m = fmaxf(__uint_as_float(a[0]), __uint_as_float(a[1]));
  auto c = __builtin_amdgcn_permlane32_swap(__float_as_uint(m), __float_as_uint(m), false, false);
  return fmaxf(__uint_as_float(c[0]), __uint_as_float(c[1]));
}
DI float xsum4(float m) {
  auto a = __builtin_amdgcn_permlane16_swap(__float_as_uint(m), __float_as_uint(m), false, false);
  m = __uint_as_float(a[0]) + __uint_as_float(a[1]);
  auto c = __builtin_amdgcn_permlane32_swap(__float_as_uint(m), __float_as_uint(m), false, false);
  return __uint_as_float(c[0]) + __uint_as_float(c[1]);
}

template <int IMM> DI void dma0(unsigned ldsaddr, unsigned voff, rsrc_t rs, unsigned soff) {
  asm volatile("s_mov_b32 m0, %0\n\ts_nop 0\n\tbuffer_load_dwordx4 %1, %2, %3 offen offset:%4 lds" ::"s"(ldsaddr), "v"(voff), "s"(rs), "s"(soff), "i"(IMM) : "memory");
}
template <int IMM> DI void dmaN(unsigned voff, rsrc_t rs, unsigned soff) {
  asm volatile("buffer_load_dwordx4 %0, %1, %2 offen offset:%3 lds" ::"v"(voff), "s"(rs), "s"(soff), "i"(IMM) : "memory");
}
DI void spacer(unsigned& dummy, rsrc_t rz) { asm volatile("buffer_load_dword %0, off, %1, 0" : "+{v255}"(dummy) : "s"(rz) : "memory"); }
DI void dma_unit(unsigned ldsaddr, const unsigned (&v)[4], rsrc_t rs, unsigned soff) {
  dma0<0>(ldsaddr, v[0], rs, soff); dmaN<1024>(v[1], rs, soff); dmaN<2048>(v[2], rs, soff); dmaN<3072>(v[3], rs, soff);
}
template <int NT, class MID>
DI void oproj(const bf16x8 (&af)[8][NT], const unsigned (&ob)[4], rsrc_t rsS, unsigned sbase, unsigned shalf, MID mid) {
  bf16x8 bf[2][8];
  f32x4 acc[2][NT];
  u32x2 keep = {0u, 0u};
#pragma unroll
  for (int j = 0; j < 8; ++j) bf[0][j] = lds128(ob[j >> 1] + (j & 1) * 5120);
  SBAR();
#pragma unroll
  for (int rt = 0; rt <= 5; ++rt) {
    const int c = rt & 1, p = c ^ 1;
    u32x4 d;
    if (rt == 5) MFMA_DRAIN();
#pragma unroll
    for (int i = 0; i < 8 * NT; ++i) {
      const int j = i / NT, n = i % NT;
      if (rt < 5) { if (j == 0) mfma_init(acc[c][n], af[j][n], bf[c][j]); else mfma_acc(acc[c][n], af[j][n], bf[c][j]); }
      if (rt < 4 && n == 1) bf[p][j] = lds128(ob[j >> 1] + (j & 1) * 5120 + (rt + 1) * 1024);
      if (rt > 0) {
        const int r = rt - 1;
        if (i == 8) { d[0] = cvtpk(acc[p][0][0], acc[p][0][1]); d[1] = cvtpk(acc[p][0][2], acc[p][0][3]); }
        if (i == 9) { d[2] = cvtpk(acc[p][1][0], acc[p][1][1]); d[3] = cvtpk(acc[p][1][2], acc[p][1][3]); }
        if (i == 11) __builtin_amdgcn_raw_buffer_store_b128(d, rsS, sbase + (NT == 4 ? r * 2048 : r * 1024), 0, 16);
        if (NT == 4) {
          if (i == 14) { d[0] = cvtpk(acc[p][2][0], acc[p][2][1]); d[1] = cvtpk(acc[p][2][2], acc[p][2][3]); }
          if (i == 15) { d[2] = cvtpk(acc[p][NT - 1][0], acc[p][NT - 1][1]); d[3] = cvtpk(acc[p][NT - 1][2], acc[p][NT - 1][3]); }
          if (i == 18) __builtin_amdgcn_raw_buffer_store_b128(d, rsS, sbase + r * 2048 + 1024, 0, 16);
        } else {
          if (i == 14) {
            const u32x2 e = {cvtpk(acc[p][2][0], acc[p][2][1]), cvtpk(acc[p][2][2], acc[p][2][3])};
            if (r & 1) d = u32x4{keep[0], keep[1], e[0], e[1]}; else keep = e;
          }
          if (i == 17 && (r & 1)) __builtin_amdgcn_raw_buffer_store_b128(d, rsS, sbase + (5 + (r >> 1)) * 1024, 0, 16);
          if (i == 17 && r == 4) __builtin_amdgcn_raw_buffer_store_b64(keep, rsS, shalf, 0, 16);
        }
      }
      SBAR();
    }
    if (rt == 0) mid();
  }
}
DI void zero_acc(f32x4 (&acc)[4][5]) {
#pragma unroll
  for (int dt = 0; dt < 4; ++dt)
#pragma unroll
    for (int rt = 0; rt < 5; ++rt) acc[dt][rt] = f32x4{0.f, 0.f, 0.f, 0.f};
}
DI float read_D(int rt, unsigned fl, int row) {
  float d[4];
#pragma unroll
  for (int w = 0; w < 4; ++w) {
    const float v = ldsf(L_DLT + (w * 80 + rt * 16 + row) * 4);
    d[w] = ((fl >> (8 * w)) & 1) ? v : 0.f;
  }
  return fmaxf(fmaxf(d[0], d[1]), fmaxf(d[2], d[3]));
}
DI float fadd_s(float a, float b) { float r = a + b; asm("" : "+v"(r)); return r; }
DI unsigned ls_addr(int w, int rt, int row) { return (rt < 4 ? L_K0 + rt * 64 : L_K1) + w * KSLOT + 8192 + row * 4; }

constexpr unsigned long long mkord(const int (&o)[16]) { unsigned long long r = 0; for (int i = 0; i < 16; ++i) r |= (unsigned long long)o[i] << (4 * i); return r; }
constexpr int ORD0[16] = {0, 4, 1, 2, 3, 8, 5, 6, 7, 12, 9, 10, 11, 13, 14, 15};
constexpr int ORD1[16] = {1, 0, 5, 2, 3, 4, 9, 6, 7, 8, 13, 10, 11, 12, 14, 15};
constexpr int ORD2[16] = {2, 0, 1, 6, 3, 4, 5, 10, 7, 8, 9, 14, 11, 12, 13, 15};
constexpr int ORD3[16] = {3, 0, 1, 2, 7, 4, 5, 6, 11, 8, 9, 10, 15, 12, 13, 14};
#define INL __attribute__((always_inline))

__global__ __launch_bounds__(512, 2) void gqa_fused(const void* __restrict__ x, const void* __restrict__ kc, const void* __restrict__ vc,
                                                    const void* __restrict__ wq, const void* __restrict__ wo, char* ws, char* __restrict__ out) {
  unsigned long long stamp[NSTAMP] = {};
  STAMP(0);
  unsigned long long cyc0 = __builtin_amdgcn_s_memtime(), cycB = 0;
  TA(0);
  const int wid = __builtin_amdgcn_readfirstlane(threadIdx.x >> 6);
  if (wid >= 4) {
    unsigned long long pc;
    asm volatile("s_getpc_b64 %0" : "=s"(pc));
    const rsrc_t rsC = __builtin_amdgcn_make_buffer_rsrc((void*)(pc & ~127ull), 0, CODE_BYTES, 0x00020000);
    asm volatile("s_mov_b32 m0, 0x8000\n\ts_nop 4\n\tbuffer_load_dword %0, %1, %2 offen lds" ::"v"((threadIdx.x & 63) * 128), "s"(rsC), "s"((wid - 4) * 8192) : "memory");
  }
  SBAR();
  const int bid = blockIdx.x, kvh = bid & 7, q = (bid >> 3) & 3, b = bid >> 5, hg = q >> 1, ph = q & 1, h = 2 * kvh + hg;
  const int w = wid & 3, hf = wid >> 2;
  const unsigned ks0 = L_K0 + w * KSLOT, ks1 = L_K1 + w * KSLOT, vra = L_VR + w * 16384, vrb = vra + 8192;
  const unsigned long long ord = q == 0 ? mkord(ORD0) : q == 1 ? mkord(ORD1) : q == 2 ? mkord(ORD2) : mkord(ORD3);
#define TILE(t) ((unsigned)(ord >> (4 * (t))) & 15u)
  const rsrc_t rsZ = __builtin_amdgcn_make_buffer_rsrc((char*)wo, 0, 0, 0x00020000);
  const rsrc_t rsK = __builtin_amdgcn_make_buffer_rsrc((char*)kc + (size_t)(b * HKV + kvh) * SK * KVROW - 128, 0, ABL_KSZ(SK * KVROW + 128), 0x00020000);
  const rsrc_t rsV = __builtin_amdgcn_make_buffer_rsrc((char*)vc + (size_t)(b * HKV + kvh) * SK * KVROW, 0, ABL_VSZ(SK * KVROW), 0x00020000);
  unsigned dummy = 0;
#define SP() spacer(dummy, rsZ)
#define DMAK(P, slot, so)                                                                        \
  do {                                                                                           \
    if (((P) & 3) == 0) dma0<0>((slot) + ((P) >> 2) * 4224, kv0, rsK, (so) + ((P) >> 2) * 4096); \
    else dmaN<((P) & 3) * 1056>(kv0, rsK, (so) + ((P) >> 2) * 4096 - ((P) & 3) * 32);            \
  } while (0)

  bf16x8 kf[8];
  unsigned kv0, kra;
  {
    const rsrc_t rsX = __builtin_amdgcn_make_buffer_rsrc((char*)x + (size_t)(b * SQ + ph * 80) * XROW, 0, 80 * XROW, 0x00020000);
    const rsrc_t rsWQ = __builtin_amdgcn_make_buffer_rsrc((char*)wq, 0, DM * WQROW, 0x00020000);
    const int lane = lane_now();
    unsigned wv[4];
    unit_voff<true>(wv, lane, WQROW);
    const unsigned xv = (lane >> 3) * XROW + ((unsigned)((lane & 7) ^ (lane >> 4) ^ ((wid & 1) << 2)) << 4);
    const unsigned rot = (unsigned)(2 * b + ph) % 7u;
    auto chunk = [&](int s) INL { const unsigned c = rot + s; return c >= 7 ? c - 7 : c; };
    constexpr unsigned XS[7] = {L_K0, L_K0 + 10240, L_K0 + 20480, L_P0, L_P1, L_MS, L_P0};
    auto aslot = [&](int s) INL { return s % 3 == 0 ? vrb : s % 3 == 1 ? vra : ks1; };
    const unsigned wso = hf * 32 * WQROW + h * 512 + w * 128;
    auto req_x = [&](int s) INL {
      dma0<0>(XS[s] + wid * 1024, xv, rsX, wid * 8 * XROW + chunk(s) * 128);
      if (wid < 2) dma0<0>(XS[s] + (wid + 8) * 1024, xv, rsX, (wid + 8) * 8 * XROW + chunk(s) * 128);
    };
    TA(10);
    if (TOUCH == 2 && wid == 3) {
      const rsrc_t rsW = __builtin_amdgcn_make_buffer_rsrc(ws, 0, WS_SLAB + 256 * SLAB, 0x00020000);
      const int l = lane_now(), grp = b * 2 + ph;
      const unsigned o = l < 8 ? WS_FLAG + (grp * 8 + l) * 128 : l < 16 ? WS_EPOCH + (grp * 8 + l - 8) * 128 : l < 32 ? WS_SLAB + (grp * 16 + l - 16) * SLAB : 0x80000000u;
      asm volatile("buffer_load_dword %0, %1, %2, 0 offen sc1" : "+{v255}"(dummy) : "v"(o), "s"(rsW) : "memory");
    }
    req_x(0); dma_unit(aslot(0) + hf * 4096, wv, rsWQ, chunk(0) * 64 * WQROW + wso);
    asm volatile("s_barrier" ::: "memory");
    req_x(1); dma_unit(aslot(1) + hf * 4096, wv, rsWQ, chunk(1) * 64 * WQROW + wso);
    TA(1);
    kv0 = (lane >> 5) * 512 + (((lane & 31) ^ (lane >> 5)) << 4);
    kra = ks0 + ((lane & 15) >> 1) * 1056 + (lane & 1) * 512 + (((lane >> 4) ^ (lane & 1)) << 4);
    unsigned tv[2], tk[2], xk[2], xp[2];
#pragma unroll
    for (int i = 0; i < 2; ++i) {
      const unsigned o = off_tr<true>(lane, 2 * hf + i);
      tv[i] = vra + o; tk[i] = ks1 + o; xk[i] = off_x(lane) ^ (64 * i); xp[i] = L_P0 + xk[i];
      asm volatile("" : "+v"(tv[i]), "+v"(tk[i]), "+v"(xk[i]), "+v"(xp[i]));
    }
    f32x4 acc[2][5];
    bf16x8 a0[2], b0[5], a1[2], b1[5];
    auto stepA = [&](int mma, int RS, int RH, int DS, int DH, bf16x8 (&ca)[2], bf16x8 (&cb)[5], bf16x8 (&na)[2], bf16x8 (&nb)[5]) INL {
#pragma unroll
      for (int i = 0; i < 10; ++i) {
        if (mma == 1) mfma_acc(acc[i & 1][i >> 1], ca[i & 1], cb[i >> 1]);
        if (mma == 2) mfma_init(acc[i & 1][i >> 1], ca[i & 1], cb[i >> 1]);
        if (RS >= 0) {
          const unsigned xa = RS < 3 ? xk[RH] + XS[RS] : xp[RH] + (XS[RS] - L_P0);
          if (i == 0 || i == 2) na[i >> 1] = ldfrag<true>((RS % 3 == 2 ? tk[i >> 1] : tv[i >> 1] + (RS % 3 == 0 ? 8192 : 0)) + RH * 4096);
          if (i == 1) nb[0] = lds128(xa);
          if (i >= 3 && i <= 6) nb[i - 2] = lds128(xa + (i - 2) * 2048);
        }
        if (DS >= 0) {
          const unsigned so = chunk(DS) * 64 * WQROW + wso;
          if (i == 7) { if (DH == 0) dma0<0>(aslot(DS) + hf * 4096, wv[0], rsWQ, so); else dmaN<2048>(wv[2], rsWQ, so); }
          if (i == 9) { if (DH == 0) dmaN<1024>(wv[1], rsWQ, so); else dmaN<3072>(wv[3], rsWQ, so); }
        }
        SBAR();
      }
    };
    if (wid < 2) WAITVM(6); else WAITVM(5);
    BARRIER();
    TA(2);
    STAMP(1);
    TB(0);
    stepA(false, 0, 0, -1, 0, a1, b1, a0, b0);
    {
#pragma unroll
      for (int i = 0; i < 5; ++i) {
        const unsigned p = wid + 8 * i, cs = p / 10, j = p - cs * 10;
        const unsigned sl = cs == 0 ? XS[2] : L_P0 + (cs - 1) * 10240;
        unsigned c = rot + 2 + cs; c = c >= 7 ? c - 7 : c;
        dma0<0>(sl + j * 1024, xv, rsX, j * 8 * XROW + c * 128);
      }
      const unsigned so = chunk(2) * 64 * WQROW + wso;
      dma0<0>(aslot(2) + hf * 4096, wv[0], rsWQ, so); dmaN<1024>(wv[1], rsWQ, so);
      SBAR();
    }
    TB(1);
    stepA(2, 0, 1, 2, 1, a0, b0, a1, b1);
    TB(2);
    WAITVM(9);
    BARRIER();
    TB(3);
#pragma unroll
    for (int s = 1; s < 5; ++s) {
      if (s == 4) { req_x(6); SBAR(); }
      stepA(true, s, 0, s + 2, 0, a1, b1, a0, b0);
      if (s == 3) TB(10);
      stepA(true, s, 1, s + 2, 1, a0, b0, a1, b1);
      if (s == 1) TB(4);
      if (s == 2) TB(7);
      if (s == 3) TB(11);
      WAITVM(4);
      if (s == 1) TB(5);
      if (s == 2) TB(8);
      if (s == 3) TB(12);
      BARRIER();
      if (s == 1) TB(6);
      if (s == 2) TB(9);
      if (s == 3) TB(13);
      if (s == 4) TB(14);
    }
    TA(3);
    if (hf == 0) {
      const unsigned so = TILE(0) * 32768 + w * 8192 + 128;
      DMAK(0, ks0, so); DMAK(1, ks0, so); DMAK(2, ks0, so); DMAK(3, ks0, so);
      DMAK(4, ks0, so); DMAK(5, ks0, so); DMAK(6, ks0, so); DMAK(7, ks0, so);
    } else {
      unit_voff<false>(wv, lane, KVROW);
      dma_unit(vra, wv, rsV, TILE(0) * 32768 + w * 128);
      dma_unit(vra + 4096, wv, rsV, TILE(0) * 32768 + 16384 + w * 128);
    }
    SBAR();
    stepA(true, 5, 0, -1, 0, a1, b1, a0, b0);
    stepA(true, 5, 1, -1, 0, a0, b0, a1, b1);
    WAITVM(8);
    BARRIER();
    stepA(true, 6, 0, -1, 0, a1, b1, a0, b0);
    stepA(true, 6, 1, -1, 0, a0, b0, a1, b1);
    if (hf == 0) {
      WAITVM(0);
#pragma unroll
      for (int ks = 0; ks < 8; ++ks) kf[ks] = lds128(kra + ks * 64);
      SBAR();
    }
    stepA(true, -1, 0, -1, 0, a1, b1, a0, b0);
    TA(4);
    MFMA_DRAIN();
    const int ksq = 2 * w + hf, g = lane >> 4;
    const unsigned qa = (ksq < 6 ? L_K1 + ksq * 5120 : L_P1 + (ksq - 6) * 5120) + ((g >> 1) * 16 + (lane & 15)) * 16 + (g & 1) * 8;
#pragma unroll
    for (int rt = 0; rt < 5; ++rt)
#pragma unroll
      for (int u = 0; u < 2; ++u) {
        const f32x4 v = acc[u][rt] * QSCALE;
        *(LAS u32x2*)(qa + rt * 1024 + u * 512) = u32x2{cvtpk(v[0], v[1]), cvtpk(v[2], v[3])};
        SBAR();
      }
    if (wid == 4 && lane < 32) *(LAS unsigned*)(L_FLAG + lane * 4) = 0u;
    WAITLGKM0();
    TA(11);
    if (hf == 0) {
      const unsigned so = TILE(1) * 32768 + w * 8192 + 128;
      DMAK(0, ks0, so); DMAK(1, ks0, so); SP(); DMAK(2, ks0, so); DMAK(3, ks0, so); SP();
      DMAK(4, ks0, so); DMAK(5, ks0, so); SP(); DMAK(6, ks0, so); DMAK(7, ks0, so); SP();
    }
    TA(5);
    asm volatile("s_barrier" ::: "memory");
    TBSAVE(true);
    TA(6);
    STAMP(2);
  }

  const unsigned rotC = (unsigned)(2 * b + ph) & 3u;
  const rsrc_t rsWO = __builtin_amdgcn_make_buffer_rsrc((char*)wo + (size_t)h * DH * WOROW, 0, wid == 7 ? 0 : DH * WOROW, 0x00020000);
  unsigned long long epoch;
  gu64* const epw = (gu64*)(ws + WS_EPOCH + ((b * 2 + ph) * 8 + wid) * 128);
  const rsrc_t rsS = __builtin_amdgcn_make_buffer_rsrc(ws + WS_SLAB + (size_t)((b * 2 + ph) * 16 + h) * SLAB, 0, SLAB, 0x00020000);
  auto o_bases = [&](unsigned (&ob)[4], int lane) INL {
#pragma unroll
    for (int i = 0; i < 4; ++i) { const unsigned cc = (rotC + i) & 3; ob[i] = (cc == 3 ? L_OI3 : L_P0 + cc * 10240) + lane * 16; }
  };

  if (wid < 4) {
    unsigned pwa;
    bf16x8 qf[5][8];
    {
      const int lane = lane_now();
      pwa = L_P0 + (w >> 1) * 5120 + (PLAY == 0 ? (w & 1) * 512 + lane * 8 : lane * 16 + (w & 1) * 8);
      unsigned q0 = L_K1 + lane * 16, q1 = L_P1 + lane * 16;
      asm volatile("" : "+v"(q0), "+v"(q1));
#pragma unroll
      for (int rt = 0; rt < 5; ++rt)
#pragma unroll
        for (int ks = 0; ks < 8; ++ks) qf[rt][ks] = lds128((ks < 6 ? q0 + ks * 5120 : q1 + (ks - 6) * 5120) + rt * 1024);
      SBAR();
    }
    float lsum[5] = {0.f, 0.f, 0.f, 0.f, 0.f}, tsr[5] = {0.f, 0.f, 0.f, 0.f, 0.f};
    f32x4 S0 = {0.f, 0.f, 0.f, 0.f};
#pragma unroll
    for (int ks = 0; ks < 8; ++ks) S0 = mfma(kf[ks], qf[0][ks], S0);
    TA(7);
    unsigned dslot = ks1;
    const unsigned pwsum = 2 * pwa + 10240, krsum = 2 * kra + L_K1;
    auto qk_tile = [&](int t, auto wo_c) INL -> unsigned {
      constexpr bool WO = decltype(wo_c)::value;
      unsigned so, wv[4];
      if constexpr (WO) { unit_voff<false>(wv, lane_now(), WOROW); so = ((rotC + t - 14) & 3) * 64 * WOROW + w * 128; }
      else so = TILE(t + 2) * 32768 + w * 8192 + 128;
      unsigned fa = L_FLAG + t * 4;
      asm volatile("" : "+v"(fa));
      asm volatile("" : "+s"(so));
      TR(11);
      BARRIER();
      TR(12);
      TRSAVE(t == TRT + 1);
      if (t == 1) { STAMP(3); cycB = __builtin_amdgcn_s_memtime(); }
      if (t == 13) { STAMP(4); stamp[10] = stamp[0] + (__builtin_amdgcn_s_memtime() - cycB) / 120; }
      if (t == 0) TA(8);
      if (t == 1) { TA(9); TASAVE(true); }
      unsigned fl;
      f32x4 S1;
      float p0, p1, p2, p3, tm;
      u32x2 pk;
#define QDMA(P)                                                                                                   \
  do {                                                                                                            \
    if constexpr (WO) {                                                                                           \
      if (((P) & 3) == 0) dma0<0>(dslot + ((P) >> 2) * 4096, wv[0], rsWO, so + ((P) >> 2) * 32 * WOROW);           \
      else dmaN<((P) & 3) * 1024>(wv[(P) & 3], rsWO, so + ((P) >> 2) * 32 * WOROW);                                \
    } else DMAK(P, dslot, so);                                                                                    \
  } while (0)
#define QM(i, RN, SN) if ((i) == 0) mfma_init(SN, kf[i], qf[RN][i]); else mfma_acc(SN, kf[i], qf[RN][i]);
#define QKBURST(RC, SC, RN, SN)                                                                                    \
  TR(2 * RC); QM(0, RN, SN) SBAR(); PRIO_HI(); p0 = ex2(SC[0]); SBAR();                                            \
  QM(1, RN, SN) SBAR(); p1 = ex2(SC[1]); SBAR();                                                                   \
  QM(2, RN, SN) SBAR(); p2 = ex2(SC[2]); SBAR();                                                                   \
  QM(3, RN, SN) SBAR(); p3 = ex2(SC[3]); SBAR();                                                                   \
  QM(4, RN, SN) SBAR(); pk[0] = cvtpk(p0, p1); p0 = fadd_s(p0, p1); SBAR();                                        \
  QM(5, RN, SN) SBAR(); pk[1] = cvtpk(p2, p3); p2 = fadd_s(p2, p3); SBAR();                                        \
  QM(6, RN, SN) SBAR(); lsum[RC] = fadd_s(lsum[RC], tsr[RC]); tsr[RC] = fadd_s(p0, p2); SBAR();                    \
  QM(7, RN, SN) SBAR(); PRIO_LO(); TR(2 * RC + 1);
#define QKFIN(RC)                                                                                                  \
  *(LAS u32x2*)(pwa + RC * 1024) = pk;                                                                             \
  tm = RC == 0 ? tsr[0] : fadd_s(tm, tsr[RC]); SBAR();
      QKBURST(0, S0, 1, S1)
      fl = *(LAS unsigned*)fa;
      QKFIN(0)
      QDMA(0); QDMA(1); SP(); SBAR();
      QKBURST(1, S1, 2, S0)
      QKFIN(1)
      unsigned long long bad = __ballot(fl != 0);
      QDMA(2); QDMA(3); SP(); SBAR();
      QKBURST(2, S0, 3, S1)
      QKFIN(2)
      QDMA(4); QDMA(5); SP(); SBAR();
      QKBURST(3, S1, 4, S0)
      WAITVM(10);
#pragma unroll
      for (int ks = 0; ks < 8; ++ks) kf[ks] = lds128(kra + ks * 64);
      kra = krsum - kra;
      asm volatile("" : "+v"(kra));
      SBAR();
      QKFIN(3)
      QKBURST(4, S0, 0, S1)
      QKFIN(4)
      bad |= __ballot(!(tm <= PTHR));
      pwa = pwsum - pwa;
      asm volatile("" : "+v"(pwa));
      SBAR();
      QDMA(6); QDMA(7); SP(); SBAR();
      dslot = ks0 + ks1 - dslot;
      S0 = S1;
#undef QKFIN
#undef QKBURST
#undef QM
#undef QDMA
      if (!WO && __builtin_expect(t == 0, 0)) bad |= __ballot(!(fminf(fminf(fminf(tsr[0], tsr[1]), fminf(tsr[2], tsr[3])), tsr[4]) >= PLOW));
      TR(10);
      return (unsigned)bad | (unsigned)(bad >> 32);
    };
    int t = -1;
    unsigned abn, last = 0x4000u;
#pragma unroll 1
    do { ++t; last >>= 1; asm volatile("" : "+s"(last)); abn = qk_tile(t, std::false_type{}); } while ((abn | (last & 1u)) == 0);
    if (abn == 0) {
#pragma unroll 1
      do { ++t; abn = qk_tile(t, std::true_type{}); } while ((abn | (t & 1)) == 0);
    }
    int entry = 2;
    if (abn == 0) {
#pragma unroll
      for (int rt = 0; rt < 5; ++rt) lsum[rt] += tsr[rt];
      t = 16;
      BARRIER();
      entry = __builtin_amdgcn_readfirstlane(*(LAS unsigned*)(L_FLAG + 64)) != 0;
    } else {
      kra = krsum - kra;
      pwa = pwsum - pwa;
    }
    if (__builtin_expect(entry != 0, 0)) {
      MFMA_DRAIN();
      float negm[5] = {0.f, 0.f, 0.f, 0.f, 0.f};
#pragma unroll 1
      for (;; ++t) {
        if (entry == 0) BARRIER();
        {
          const unsigned fl = __builtin_amdgcn_readfirstlane(*(LAS unsigned*)(L_FLAG + t * 4));
          if (fl != 0) {
            const unsigned pprev = pwsum - pwa;
            const int row = lane_now() & 15;
#pragma unroll
            for (int rt = 0; rt < 5; ++rt) {
              const float D = read_D(rt, fl, row);
              const float own = ((fl >> (8 * w)) & 1) ? ldsf(L_DLT + (w * 80 + rt * 16 + row) * 4) : 0.f;
              const float f = ex2(own - D);
              const u32x2 pk = *(LAS u32x2*)(pprev + rt * 1024);
              const float a0 = __uint_as_float(pk[0] << 16) * f, a1 = __uint_as_float(pk[0] & 0xffff0000u) * f;
              const float a2 = __uint_as_float(pk[1] << 16) * f, a3 = __uint_as_float(pk[1] & 0xffff0000u) * f;
              *(LAS u32x2*)(pprev + rt * 1024) = u32x2{cvtpk(a0, a1), cvtpk(a2, a3)};
              lsum[rt] *= f;
              negm[rt] -= D;
              SBAR();
            }
            BARRIER();
          }
        }
        if (t == 16) break;
        if (entry != 2) {
          const unsigned dslot = (t & 1) ? ks0 : ks1;
          if (t < 14) {
            const unsigned so = TILE(t + 2) * 32768 + w * 8192 + 128;
            DMAK(0, dslot, so); DMAK(1, dslot, so); DMAK(2, dslot, so); DMAK(3, dslot, so);
            DMAK(4, dslot, so); DMAK(5, dslot, so); DMAK(6, dslot, so); DMAK(7, dslot, so);
          } else {
            unsigned wv[4];
            unit_voff<false>(wv, lane_now(), WOROW);
            const unsigned so = ((rotC + t - 14) & 3) * 64 * WOROW + w * 128;
            dma_unit(dslot, wv, rsWO, so);
            dma_unit(dslot + 4096, wv, rsWO, so + 32 * WOROW);
          }
        } else {
          const int lane = lane_now();
#pragma unroll
          for (int ks = 0; ks < 8; ++ks)
            kf[ks] = __builtin_bit_cast(bf16x8, __builtin_amdgcn_raw_buffer_load_b128(rsK, (lane & 15) * 512 + (lane >> 4) * 16 + ks * 64, TILE(t) * 32768 + w * 8192 + 128, 0));
        }
        f32x4 S[5];
        float mx[5];
        bool need = false;
#pragma unroll
        for (int rt = 0; rt < 5; ++rt) {
          S[rt] = f32x4{negm[rt], negm[rt], negm[rt], negm[rt]};
#pragma unroll
          for (int ks = 0; ks < 8; ++ks) S[rt] = mfma(kf[ks], qf[rt][ks], S[rt]);
          mx[rt] = ceilf(xmax4(fmaxf(fmaxf(S[rt][0], S[rt][1]), fmaxf(S[rt][2], S[rt][3]))));
          need |= !(mx[rt] <= GTHR) || (t == 0 && !(mx[rt] >= -GTHR));
          SBAR();
        }
        need = __any(need);
        const int lane = lane_now();
#pragma unroll
        for (int rt = 0; rt < 5; ++rt) {
          const float dl = need ? (t > 0 ? fmaxf(mx[rt], 0.f) : mx[rt]) : 0.f;
          const float p0 = ex2(S[rt][0] - dl), p1 = ex2(S[rt][1] - dl), p2 = ex2(S[rt][2] - dl), p3 = ex2(S[rt][3] - dl);
          const float ts = (p0 + p1) + (p2 + p3);
          lsum[rt] = t > 0 ? lsum[rt] * ex2(-dl) + ts : ts;
          *(LAS u32x2*)(pwa + rt * 1024) = u32x2{cvtpk(p0, p1), cvtpk(p2, p3)};
          if (need && lane < 16) *(LAS float*)(L_DLT + (w * 80 + rt * 16 + lane) * 4) = dl;
          SBAR();
        }
        if (need) *(LAS unsigned char*)(L_FLAG + (t + 1) * 4 + w) = 1;
        WAITVM(8);
#pragma unroll
        for (int ks = 0; ks < 8; ++ks) kf[ks] = lds128(kra + ks * 64);
        kra = krsum - kra;
        pwa = pwsum - pwa;
        entry = 0;
      }
    }
    {
      const int lane = lane_now();
#pragma unroll
      for (int rt = 0; rt < 5; ++rt) {
        const float l = xsum4(lsum[rt]);
        if (lane < 16) *(LAS float*)ls_addr(w, rt, lane) = l;
      }
    }
    const int lane = lane_now();
    unsigned t1[4], wv[4], ob[4];
#pragma unroll
    for (int dt = 0; dt < 4; ++dt) t1[dt] = ks0 + off_tr<false>(lane, dt);
    unit_voff<false>(wv, lane, WOROW);
    o_bases(ob, lane);
    bf16x8 af[8][4];
    const unsigned so2 = ((rotC + 2) & 3) * 64 * WOROW, so3 = ((rotC + 3) & 3) * 64 * WOROW;
    WAITVM(8);
#pragma unroll
    for (int j = 0; j < 2; ++j)
#pragma unroll
      for (int dt = 0; dt < 4; ++dt) af[j][dt] = ldfrag<false>(t1[dt] + L_K1 + j * 4096);
    WAITLGKM0();
    dma_unit(ks1, wv, rsWO, so2 + w * 128); SP();
    dma_unit(ks1 + 4096, wv, rsWO, so2 + 32 * WOROW + w * 128); SP();
    WAITVM(10);
#pragma unroll
    for (int j = 2; j < 4; ++j)
#pragma unroll
      for (int dt = 0; dt < 4; ++dt) af[j][dt] = ldfrag<false>(t1[dt] + (j & 1) * 4096);
    BARRIER();
    STAMP(5);
    dma_unit(ks0, wv, rsWO, so3 + w * 128); SP();
    dma_unit(ks0 + 4096, wv, rsWO, so3 + 32 * WOROW + w * 128); SP();
#pragma unroll
    for (int j = 4; j < 8; ++j) {
      if (w < 3) WAITVM(16); else if (j == 4) WAITVM(16); else if (j == 5) WAITVM(11); else if (j == 6) WAITVM(6); else WAITVM(1);
#pragma unroll
      for (int dt = 0; dt < 4; ++dt) af[j][dt] = ldfrag<false>(t1[dt] + (j < 6 ? L_K1 : 0) + (j & 1) * 4096);
      WAITLGKM0();
      if (w < 3) { dma_unit((j < 6 ? ks1 : ks0) + (j & 1) * 4096, wv, rsWO, (j < 6 ? so2 : so3) + (j & 1) * 32 * WOROW + (4 + w) * 128); SP(); }
    }
    asm volatile("s_barrier" ::: "memory");
    STAMP(6);
    oproj<4>(af, ob, rsS, w * 10240 + lane * 16, 0, [&]() INL {
      WAITVM(0);
      asm volatile("s_barrier" ::: "memory");
      epoch = __hip_atomic_load(epw + (lane >> 6), __ATOMIC_RELAXED, __HIP_MEMORY_SCOPE_AGENT);
    });
    STAMP(7);
  } else {
    const int lane = lane_now(), row = lane & 15;
    unsigned ta[4], dv[4];
#pragma unroll
    for (int dt = 0; dt < 4; ++dt) ta[dt] = vra + off_tr<false>(lane, dt);
    unit_voff<false>(dv, lane, KVROW);
    if (TOUCH == 1 && wid == 7) {
      const rsrc_t rsW = __builtin_amdgcn_make_buffer_rsrc(ws, 0, WS_SLAB + 256 * SLAB, 0x00020000);
      const int l = lane_now(), grp = b * 2 + ph;
      const unsigned o = l < 8 ? WS_FLAG + (grp * 8 + l) * 128 : l < 16 ? WS_EPOCH + (grp * 8 + l - 8) * 128 : l < 32 ? WS_SLAB + (grp * 16 + l - 16) * SLAB : 0x80000000u;
      asm volatile("buffer_load_dword %0, %1, %2, 0 offen sc1" : "+{v255}"(dummy) : "v"(o), "s"(rsW) : "memory");
    }
    dma_unit(vrb, dv, rsV, TILE(1) * 32768 + w * 128);
    SP();
    dma_unit(vrb + 4096, dv, rsV, TILE(1) * 32768 + 16384 + w * 128);
    SP();
    f32x4 acc[4][5];
    zero_acc(acc);
    bf16x8 va[2][4], vb[2][4];
    WAITVM(10);
#pragma unroll
    for (int hh = 0; hh < 2; ++hh)
#pragma unroll
      for (int dt = 0; dt < 4; ++dt) va[hh][dt] = ldfrag<false>(ta[dt] + hh * 4096);
    unsigned pa = L_P0 + lane * (PLAY == 0 ? 8 : 16);
    asm volatile("" : "+v"(pa));
    TA(7);
    BARRIER();
    TA(8);
    TASAVE(true);
    rsrc_t rsD = rsV;
    unsigned dstep = 16384;
    auto pv_tile = [&](int t, auto par_c, bf16x8 (&vc)[2][4], bf16x8 (&vn)[2][4]) INL {
      constexpr int PAR = decltype(par_c)::value;
      unsigned so;
      if (t < 14) so = TILE(t + 2) * 32768 + w * 128;
      else so = ((rotC + t - 14) & 3) * 64 * WOROW + wid * 128;
      const unsigned dsl = vra + PAR * 8192;
      unsigned fa = L_FLAG + (t + 1) * 4;
      asm volatile("" : "+v"(fa));
      asm volatile("" : "+s"(so));
      bf16x8 pf[5][2];
#define RDP(rt) { pf[rt][0] = ldp(pa + PAR * 10240 + (rt) * 1024); pf[rt][1] = ldp(pa + PAR * 10240 + 5120 + (rt) * 1024); }
#define PM(i, rt) mfma_acc(acc[(i) & 3][rt], vc[(i) >> 2][(i) & 3], pf[rt][(i) >> 2]);
#define RDV(dt, hh) vn[hh][dt] = ldfrag<false>(ta[dt] + (PAR ^ 1) * 8192 + (hh) * 4096);
#define BURST(rt)                                                                                                  \
  SBAR(); TR(2 * rt + 1); PM(0, rt) SBAR(); PRIO_HI(); SBAR(); PM(1, rt) PM(2, rt) PM(3, rt) PM(4, rt) PM(5, rt) PM(6, rt) PM(7, rt) SBAR(); PRIO_LO(); TR(2 * rt + 2); SBAR();
#define PD(P)                                                                                                      \
  {                                                                                                                \
    if (((P) & 3) == 0) dma0<0>(dsl + ((P) >> 2) * 4096, dv[0], rsD, (P) ? so + dstep : so);                       \
    else dmaN<((P) & 3) * 1024>(dv[(P) & 3], rsD, (P) >> 2 ? so + dstep : so);                                     \
  }
      TR(11);
      BARRIER();
      TR(12);
      TRSAVE(t == TRT);
      TR(0);
      unsigned fl = *(LAS unsigned*)fa;
      RDP(0) RDP(1)
      SBAR();
      PD(0) SP();
      SBAR();
      fl = __builtin_amdgcn_readfirstlane(fl);
      if (__builtin_expect(fl != 0, 0)) {
        if (t > 0) {
#pragma unroll
          for (int rt = 0; rt < 5; ++rt) {
            const float f = ex2(-read_D(rt, fl, row));
#pragma unroll
            for (int dt = 0; dt < 4; ++dt) acc[dt][rt] *= f;
          }
        }
        BARRIER();
        RDP(0) RDP(1)
      }
      BURST(0)
      RDP(2) SBAR();
      PD(1) PD(2) SP();
      BURST(1)
      RDP(3) SBAR();
      WAITVM(11);
      RDV(0, 0) RDV(1, 0) SBAR();
      PD(3)
      BURST(2)
      RDP(4) RDV(2, 0) RDV(3, 0) SBAR();
      PD(4) PD(5) SP();
      if (PAR == 1 && t == 15) BARRIER();
      BURST(3)
      WAITVM(10);
      RDV(0, 1) RDV(1, 1) RDV(2, 1) RDV(3, 1) SBAR();
      PD(6) PD(7) SP();
      BURST(4)
#undef PD
#undef BURST
#undef RDV
#undef PM
#undef RDP
    };
#pragma unroll 1
    for (int t = 0; t < 16; t += 2) {
      if (t == 14) { unit_voff<false>(dv, lane_now(), WOROW); rsD = rsWO; dstep = 32 * WOROW; }
      pv_tile(t, std::integral_constant<int, 0>{}, va, vb);
      pv_tile(t + 1, std::integral_constant<int, 1>{}, vb, va);
    }
    STAMP(5);
    float inv[5];
#pragma unroll
    for (int rt = 0; rt < 5; ++rt) {
      float l = 0.f;
#pragma unroll
      for (int ww = 0; ww < 4; ++ww) l += ldsf(ls_addr(ww, rt, row));
      inv[rt] = __builtin_amdgcn_rcpf(l);
    }
    if (w == 3) WAITVM(0);
    MFMA_DRAIN();
    SBAR();
    const unsigned oa = (w == 3 ? L_OI3 : L_P0 + w * 10240) + lane * 16;
#pragma unroll
    for (int rt = 0; rt < 5; ++rt)
#pragma unroll
      for (int ks = 0; ks < 2; ++ks) {
        const f32x4 u = acc[2 * ks][rt] * inv[rt], v = acc[2 * ks + 1][rt] * inv[rt];
        *(LAS u32x4*)(oa + ks * 5120 + rt * 1024) = u32x4{cvtpk(u[0], u[1]), cvtpk(u[2], u[3]), cvtpk(v[0], v[1]), cvtpk(v[2], v[3])};
        SBAR();
      }
    unsigned ob[4], tA[3], tB[3];
    o_bases(ob, lane);
#pragma unroll
    for (int i = 0; i < 3; ++i) {
      const int T = 3 * w + i, o = T >> 2;
      const unsigned tr = off_tr<false>(lane, T & 3);
      tA[i] = L_VR + o * 16384 + tr; tB[i] = L_K0 + o * KSLOT + tr;
    }
    WAITVM(0);
    BARRIER();
    STAMP(6);
    bf16x8 af[8][3];
#pragma unroll
    for (int j = 0; j < 4; ++j)
#pragma unroll
      for (int i = 0; i < 3; ++i) af[j][i] = ldfrag<false>(tA[i] + j * 4096);
    asm volatile("s_barrier" ::: "memory");
#pragma unroll
    for (int j = 4; j < 8; ++j)
#pragma unroll
      for (int i = 0; i < 3; ++i) af[j][i] = ldfrag<false>(tB[i] + (j < 6 ? L_K1 : 0) + (j & 1) * 4096);
    oproj<3>(af, ob, rsS, 40960 + w * 7680 + lane * 16, 40960 + w * 7680 + 7168 + lane * 8, [&]() INL { epoch = __hip_atomic_load(epw + (lane >> 6), __ATOMIC_RELAXED, __HIP_MEMORY_SCOPE_AGENT); });
    STAMP(7);
  }
  {
    unsigned long long tag = epoch + 0x9E3779B97F4A7C15ull;
    tag = (tag ^ (tag >> 30)) * 0xBF58476D1CE4E5B9ull;
    tag = (tag ^ (tag >> 27)) * 0x94D049BB133111EBull;
    tag ^= tag >> 31;
    const int lane = lane_now();
    gu64* fl = (gu64*)(ws + WS_FLAG + ((b * 2 + ph) * 8 + wid) * 128);
    const int nch = hf ? 30 : 40, r = h * nch + (lane < nch ? lane : 0), piece = r >> 6;
    int nt0, nt1, rt0, rt1, l0 = r & 63, l1 = r & 63;
    if (hf == 0) {
      nt0 = 4 * w + 2 * (piece & 1); nt1 = nt0 + 1; rt0 = rt1 = piece >> 1;
    } else {
      nt0 = 16 + 3 * w + (piece < 5 ? 0 : 2); nt1 = 16 + 3 * w + (piece < 5 ? 1 : 2);
      rt0 = piece < 5 ? piece : 2 * (piece - 5); rt1 = piece < 5 ? piece : piece < 7 ? 2 * (piece - 5) + 1 : 4;
      if (piece == 7) { l0 = 2 * l0; l1 = l0 + 1; }
    }
    const unsigned d0 = ((16 * rt0 + (l0 & 15)) * DM + 16 * nt0 + 4 * (l0 >> 4)) * 2, d1 = ((16 * rt1 + (l1 & 15)) * DM + 16 * nt1 + 4 * (l1 >> 4)) * 2;
    const unsigned src = lane < nch ? (hf ? 40960 + w * 7680 : w * 10240) + r * 16 : 0x80000000u;
    const rsrc_t rsG = __builtin_amdgcn_make_buffer_rsrc(ws + WS_SLAB + (size_t)(b * 2 + ph) * 16 * SLAB, 0, 16 * SLAB, 0x00020000);
    unsigned long long f = ~tag;
    if (hf == 0) f = __hip_atomic_load(fl + (lane & 15), __ATOMIC_RELAXED, __HIP_MEMORY_SCOPE_AGENT);
    WAITVM(0);
    asm volatile("" :: "{v255}"(dummy));
    STAMP(12);
    if (lane == 0) __hip_atomic_store(fl + h, tag, __ATOMIC_RELAXED, __HIP_MEMORY_SCOPE_AGENT);
    u32x4 v[16];
#pragma unroll
    for (int hh = 0; hh < 16; ++hh) v[hh] = u32x4{0u, 0u, 0u, 0u};
    unsigned pend = 0xffffu;
#pragma unroll 1
    for (unsigned spin = 0; spin < (1u << 22); ++spin) {
      const unsigned rdy = ((unsigned)__ballot(f == tag) | (1u << h)) & pend;
      __builtin_amdgcn_fence(__ATOMIC_ACQUIRE, "wavefront");
      asm volatile("" ::: "memory");
#pragma unroll
      for (int hh = 0; hh < 16; ++hh)
        if ((rdy >> hh) & 1) v[hh] = __builtin_amdgcn_raw_buffer_load_b128(rsG, src, hh * SLAB, 16);
      pend &= ~rdy;
      if (pend == 0) break;
      if (rdy == 0) __builtin_amdgcn_s_sleep(SLP);
      f = __hip_atomic_load(fl + (lane & 15), __ATOMIC_RELAXED, __HIP_MEMORY_SCOPE_AGENT);
    }
    STAMP(13);
    if (lane < nch) {
      if (h == 0 && lane == 0) __hip_atomic_store(epw, epoch + 1, __ATOMIC_RELAXED, __HIP_MEMORY_SCOPE_AGENT);
      float sm[8] = {0.f, 0.f, 0.f, 0.f, 0.f, 0.f, 0.f, 0.f};
#pragma unroll
      for (int hh = 0; hh < 16; ++hh)
#pragma unroll
        for (int i = 0; i < 4; ++i) { sm[2 * i] += __uint_as_float(v[hh][i] << 16); sm[2 * i + 1] += __uint_as_float(v[hh][i] & 0xffff0000u); }
      char* dst = out + (size_t)(b * 2 + ph) * 80 * DM * 2;
      *(u32x2*)(dst + d0) = u32x2{cvtpk(sm[0], sm[1]), cvtpk(sm[2], sm[3])};
      *(u32x2*)(dst + d1) = u32x2{cvtpk(sm[4], sm[5]), cvtpk(sm[6], sm[7])};
    }
  }
  WAITVM(0);
  STAMP(8);
  if (wid == 0) {
    unsigned long long d = DIAG == 99 ? (__builtin_amdgcn_s_memtime() - cyc0) / 20 : DIAG == 0 ? 0 : (stamp[DIAG == 99 ? 0 : DIAG] - stamp[0]);
    while (__builtin_amdgcn_s_memrealtime() - stamp[0] < 6000 + d) __builtin_amdgcn_s_sleep(1);
  }
}

extern "C" void kernel_launch(void* const* d_in, const int* in_sizes, int n_in, void* d_out, int out_size, void* d_ws, size_t ws_size,
                              hipStream_t stream) {
  (void)in_sizes; (void)n_in; (void)out_size; (void)ws_size;
  (void)hipFuncSetAttribute((const void*)gqa_fused, hipFuncAttributeMaxDynamicSharedMemorySize, L_TOTAL);
  gqa_fused<<<256, 512, L_TOTAL, stream>>>(d_in[0], d_in[1], d_in[2], d_in[3], d_in[4], (char*)d_ws, (char*)d_out);
}
```

```cpp
#define STAMPS
#include <hip/hip_runtime.h>
#include <hip/hip_bf16.h>
#include <type_traits>
#define PP 1
#define SPT 1
#define SPCQ 1
#define SPCP 1
#define TRT 7
#define SLP 4
#define TOUCH 2
#define PLAY 0
#define CPF 1
#define ABL 0
#pragma clang diagnostic ignored "-Wint-to-pointer-cast"

typedef __bf16 bf16x8 __attribute__((ext_vector_type(8)));
typedef __bf16 bf16x4 __attribute__((ext_vector_type(4)));
typedef __bf16 bf16x2 __attribute__((ext_vector_type(2)));
typedef float f32x4 __attribute__((ext_vector_type(4)));
typedef float f32x2 __attribute__((ext_vector_type(2)));
typedef unsigned u32x4 __attribute__((ext_vector_type(4)));
typedef unsigned u32x2 __attribute__((ext_vector_type(2)));
#define LAS __attribute__((address_space(3)))
#define DI __device__ __forceinline__
typedef __amdgpu_buffer_rsrc_t rsrc_t;

constexpr int NB = 8, SQ = 160, DM = 448, HQ = 16, HKV = 8, SK = 1024, DH = 256;
constexpr unsigned XROW = DM * 2, WQROW = HQ * DH * 2, KVROW = DH * 2, WOROW = DM * 2;
constexpr unsigned SLAB = 80 * DM * 2;
constexpr unsigned WS_FLAG = 0, WS_EPOCH = 32768, WS_SLAB = 65536;
typedef __attribute__((address_space(1))) unsigned long long gu64;
constexpr unsigned KSLOT = 8448;
constexpr unsigned L_K0 = 0, L_K1 = 4 * KSLOT, L_VR = 8 * KSLOT, L_P0 = L_VR + 65536, L_P1 = L_P0 + 10240, L_MS = L_P1 + 10240, L_TOTAL = L_MS + 10240;
static_assert(L_TOTAL == 163840, "LDS");
constexpr unsigned L_FLAG = L_MS;
constexpr unsigned L_DLT = L_MS + 128;
constexpr unsigned L_OI3 = L_VR + 3 * 16384;
constexpr int CODE_BYTES = 26624;
constexpr float QSCALE = 0.0625f * 1.4426950408889634f;
#define PTHR 65536.f
#define GTHR 12.f
#define PLOW (1.f / 65536.f)

#define NSTAMP 14
#define STAMP_OFF (64u << 20)
#define STAMP(k) do { __builtin_amdgcn_sched_barrier(0); asm volatile("s_memrealtime %0\n\ts_waitcnt lgkmcnt(0)" : "=s"(stamp[k]) :: "memory"); __builtin_amdgcn_sched_barrier(0); } while (0)
#define ABL_KSZ(x) ((ABL & 1) ? 0 : (x))
#define ABL_VSZ(x) ((ABL & 1024) ? 0 : (x))
#define SBAR() __builtin_amdgcn_sched_barrier(0)
#define TRX(k) do { SBAR(); asm volatile("s_memtime %0" : "=s"(tr[k])); SBAR(); } while (0)
#define TRXSAVE(cond) do { if (cond) { WAITLGKM0(); if (lane_now() == 0) for (int k_ = 0; k_ < 16; ++k_) ((unsigned long long*)(ws + (80u << 20)))[(bid * 8 + wid) * 16 + k_] = tr[k_]; } } while (0)
#define TR(k)
#define TRSAVE(cond)
#define TB(k)
#define TBSAVE(cond)
#define TA(k)
#define TASAVE(cond)
#define PRIO_HI() __builtin_amdgcn_s_setprio(1)
#define PRIO_LO() __builtin_amdgcn_s_setprio(0)
#define WAITVM(n) asm volatile("s_waitcnt vmcnt(%0)" ::"i"(n) : "memory")
#define WAITLGKM0() do { asm volatile("" ::: "memory"); __builtin_amdgcn_s_waitcnt(0xC07F); asm volatile("" ::: "memory"); } while (0)
#define BARRIER() do { WAITLGKM0(); asm volatile("s_barrier" ::: "memory"); } while (0)

DI int lane_now() { unsigned z = 0; asm volatile("" : "+v"(z)); return __builtin_amdgcn_mbcnt_hi(~0u, __builtin_amdgcn_mbcnt_lo(~0u, z)); }
DI bf16x8 lds128(unsigned a) { return *(LAS bf16x8*)a; }
DI bf16x4 ldstr(unsigned a) { return __builtin_amdgcn_ds_read_tr16_b64_v4bf16((LAS bf16x4*)a); }
DI float ldsf(unsigned a) { return *(LAS float*)a; }
DI unsigned cvtpk(float lo, float hi) { f32x2 v = {lo, hi}; return __builtin_bit_cast(unsigned, __builtin_convertvector(v, bf16x2)); }
DI f32x4 mfma(bf16x8 a, bf16x8 b, f32x4 c) { return __builtin_amdgcn_mfma_f32_16x16x32_bf16(a, b, c, 0, 0, 0); }
DI void mfma_acc(f32x4& c, bf16x8 a, bf16x8 b) { asm volatile("v_mfma_f32_16x16x32_bf16 %0, %1, %2, %0" : "+v"(c) : "v"(a), "v"(b)); }
DI void mfma_init(f32x4& d, bf16x8 a, bf16x8 b) { asm volatile("v_mfma_f32_16x16x32_bf16 %0, %1, %2, 0" : "=&v"(d) : "v"(a), "v"(b)); }
#define MFMA_DRAIN() asm volatile("s_nop 7\n\ts_nop 1" ::: "memory")
DI float ex2(float x) { return __builtin_amdgcn_exp2f(x); }
template <bool NAT> DI void unit_voff(unsigned (&v)[4], int lane, unsigned stride) {
  const unsigned e = (lane >> 3) * stride + ((unsigned)((lane & 7) ^ (((lane >> 4) & (NAT ? 1 : 3)) << 1)) << 4);
#pragma unroll
  for (int p = 0; p < 4; ++p) v[p] = ((NAT && (p & 1)) ? e ^ 64 : e) + p * (8 * stride - 1024);
}
template <bool NAT> DI unsigned off_tr(int lane, int dt) {
  const int g = lane >> 4, qq = (lane >> 2) & 3, pp = lane & 3, f = ((qq >> 1) & 1) | ((g & 1) << 1);
  return ((NAT ? 8 : 4) * g + qq) * 128 + ((dt ^ f) << 5) + pp * 8;
}
template <bool NAT> DI bf16x8 ldfrag(unsigned a) { return __builtin_shufflevector(ldstr(a), ldstr(a + (NAT ? 512 : 2048)), 0, 1, 2, 3, 4, 5, 6, 7); }
DI unsigned off_x(int lane) { return (lane & 15) * 128 + (((lane >> 4) ^ (((lane & 15) >> 1) & 7)) << 4); }
DI bf16x8 ldp(unsigned a) {
  const bf16x4 lo = *(LAS bf16x4*)a, hi = *(LAS bf16x4*)(a + 512);
  return __builtin_shufflevector(lo, hi, 0, 1, 2, 3, 4, 5, 6, 7);
}
DI float xmax4(float m) {
  auto a = __builtin_amdgcn_permlane16_swap(__float_as_uint(m), __float_as_uint(m), false, false);
  m = fmaxf(__uint_as_float(a[0]), __uint_as_float(a[1]));
  auto c = __builtin_amdgcn_permlane32_swap(__float_as_uint(m), __float_as_uint(m), false, false);
  return fmaxf(__uint_as_float(c[0]), __uint_as_float(c[1]));
}
DI float xsum4(float m) {
  auto a = __builtin_amdgcn_permlane16_swap(__float_as_uint(m), __float_as_uint(m), false, false);
  m = __uint_as_float(a[0]) + __uint_as_float(a[1]);
  auto c = __builtin_amdgcn_permlane32_swap(__float_as_uint(m), __float_as_uint(m), false, false);
  return __uint_as_float(c[0]) + __uint_as_float(c[1]);
}

template <int IMM> DI void dma0(unsigned ldsaddr, unsigned voff, rsrc_t rs, unsigned soff) {
  asm volatile("s_mov_b32 m0, %0\n\ts_nop 0\n\tbuffer_load_dwordx4 %1, %2, %3 offen offset:%4 lds" ::"s"(ldsaddr), "v"(voff), "s"(rs), "s"(soff), "i"(IMM) : "memory");
}
template <int IMM> DI void dmaN(unsigned voff, rsrc_t rs, unsigned soff) {
  asm volatile("buffer_load_dwordx4 %0, %1, %2 offen offset:%3 lds" ::"v"(voff), "s"(rs), "s"(soff), "i"(IMM) : "memory");
}
DI void spacer(unsigned& dummy, rsrc_t rz) { asm volatile("buffer_load_dword %0, off, %1, 0" : "+{v255}"(dummy) : "s"(rz) : "memory"); }
DI void dma_unit(unsigned ldsaddr, const unsigned (&v)[4], rsrc_t rs, unsigned soff) {
  dma0<0>(ldsaddr, v[0], rs, soff); dmaN<1024>(v[1], rs, soff); dmaN<2048>(v[2], rs, soff); dmaN<3072>(v[3], rs, soff);
}
template <int NT, class MID>
DI void oproj(const bf16x8 (&af)[8][NT], const unsigned (&ob)[4], rsrc_t rsS, unsigned sbase, unsigned shalf, MID mid) {
  bf16x8 bf[2][8];
  f32x4 acc[2][NT];
  u32x2 keep = {0u, 0u};
#pragma unroll
  for (int j = 0; j < 8; ++j) bf[0][j] = lds128(ob[j >> 1] + (j & 1) * 5120);
  SBAR();
#pragma unroll
  for (int rt = 0; rt <= 5; ++rt) {
    const int c = rt & 1, p = c ^ 1;
    u32x4 d;
    if (rt == 5) MFMA_DRAIN();
#pragma unroll
    for (int i = 0; i < 8 * NT; ++i) {
      const int j = i / NT, n = i % NT;
      if (rt < 5) { if (j == 0) mfma_init(acc[c][n], af[j][n], bf[c][j]); else mfma_acc(acc[c][n], af[j][n], bf[c][j]); }
      if (rt < 4 && n == 1) bf[p][j] = lds128(ob[j >> 1] + (j & 1) * 5120 + (rt + 1) * 1024);
      if (rt > 0) {
        const int r = rt - 1;
        if (i == 8) { d[0] = cvtpk(acc[p][0][0], acc[p][0][1]); d[1] = cvtpk(acc[p][0][2], acc[p][0][3]); }
        if (i == 9) { d[2] = cvtpk(acc[p][1][0], acc[p][1][1]); d[3] = cvtpk(acc[p][1][2], acc[p][1][3]); }
        if (i == 11) __builtin_amdgcn_raw_buffer_store_b128(d, rsS, sbase + (NT == 4 ? r * 2048 : r * 1024), 0, 16);
        if (NT == 4) {
          if (i == 14) { d[0] = cvtpk(acc[p][2][0], acc[p][2][1]); d[1] = cvtpk(acc[p][2][2], acc[p][2][3]); }
          if (i == 15) { d[2] = cvtpk(acc[p][NT - 1][0], acc[p][NT - 1][1]); d[3] = cvtpk(acc[p][NT - 1][2], acc[p][NT - 1][3]); }
          if (i == 18) __builtin_amdgcn_raw_buffer_store_b128(d, rsS, sbase + r * 2048 + 1024, 0, 16);
        } else {
          if (i == 14) {
            const u32x2 e = {cvtpk(acc[p][2][0], acc[p][2][1]), cvtpk(acc[p][2][2], acc[p][2][3])};
            if (r & 1) d = u32x4{keep[0], keep[1], e[0], e[1]}; else keep = e;
          }
          if (i == 17 && (r & 1)) __builtin_amdgcn_raw_buffer_store_b128(d, rsS, sbase + (5 + (r >> 1)) * 1024, 0, 16);
          if (i == 17 && r == 4) __builtin_amdgcn_raw_buffer_store_b64(keep, rsS, shalf, 0, 16);
        }
      }
      SBAR();
    }
    if (rt == 0) mid();
  }
}
DI void zero_acc(f32x4 (&acc)[4][5]) {
#pragma unroll
  for (int dt = 0; dt < 4; ++dt)
#pragma unroll
    for (int rt = 0; rt < 5; ++rt) acc[dt][rt] = f32x4{0.f, 0.f, 0.f, 0.f};
}
DI float read_D(int rt, unsigned fl, int row) {
  float d[4];
#pragma unroll
  for (int w = 0; w < 4; ++w) {
    const float v = ldsf(L_DLT + (w * 80 + rt * 16 + row) * 4);
    d[w] = ((fl >> (8 * w)) & 1) ? v : 0.f;
  }
  return fmaxf(fmaxf(d[0], d[1]), fmaxf(d[2], d[3]));
}
DI float fadd_s(float a, float b) { float r = a + b; asm("" : "+v"(r)); return r; }
DI unsigned ls_addr(int w, int rt, int row) { return (rt < 4 ? L_K0 + rt * 64 : L_K1) + w * KSLOT + 8192 + row * 4; }

constexpr unsigned long long mkord(const int (&o)[16]) { unsigned long long r = 0; for (int i = 0; i < 16; ++i) r |= (unsigned long long)o[i] << (4 * i); return r; }
constexpr int ORD0[16] = {0, 4, 1, 2, 3, 8, 5, 6, 7, 12, 9, 10, 11, 13, 14, 15};
constexpr int ORD1[16] = {1, 0, 5, 2, 3, 4, 9, 6, 7, 8, 13, 10, 11, 12, 14, 15};
constexpr int ORD2[16] = {2, 0, 1, 6, 3, 4, 5, 10, 7, 8, 9, 14, 11, 12, 13, 15};
constexpr int ORD3[16] = {3, 0, 1, 2, 7, 4, 5, 6, 11, 8, 9, 10, 15, 12, 13, 14};
#define INL __attribute__((always_inline))

__global__ __launch_bounds__(512, 2) void gqa_fused(const void* __restrict__ x, const void* __restrict__ kc, const void* __restrict__ vc,
                                                    const void* __restrict__ wq, const void* __restrict__ wo, char* ws, char* __restrict__ out) {
  unsigned long long stamp[NSTAMP] = {};
  STAMP(0);
  unsigned long long cyc0 = __builtin_amdgcn_s_memtime(), cycB = 0;
  TA(0);
  const int wid = __builtin_amdgcn_readfirstlane(threadIdx.x >> 6);
  if (wid >= 4) {
    unsigned long long pc;
    asm volatile("s_getpc_b64 %0" : "=s"(pc));
    const rsrc_t rsC = __builtin_amdgcn_make_buffer_rsrc((void*)(pc & ~127ull), 0, CODE_BYTES, 0x00020000);
    asm volatile("s_mov_b32 m0, 0x8000\n\ts_nop 4\n\tbuffer_load_dword %0, %1, %2 offen lds" ::"v"((threadIdx.x & 63) * 128), "s"(rsC), "s"((wid - 4) * 8192) : "memory");
  }
  SBAR();
  const int bid = blockIdx.x, kvh = bid & 7, q = (bid >> 3) & 3, b = bid >> 5, hg = q >> 1, ph = q & 1, h = 2 * kvh + hg;
  const int w = wid & 3, hf = wid >> 2;
  const unsigned ks0 = L_K0 + w * KSLOT, ks1 = L_K1 + w * KSLOT, vra = L_VR + w * 16384, vrb = vra + 8192;
#define TILE(t) ((unsigned)(t))
  const rsrc_t rsZ = __builtin_amdgcn_make_buffer_rsrc((char*)wo, 0, 0, 0x00020000);
  const rsrc_t rsK = __builtin_amdgcn_make_buffer_rsrc((char*)kc + (size_t)(b * HKV + kvh) * SK * KVROW - 128, 0, ABL_KSZ(SK * KVROW + 128), 0x00020000);
  const rsrc_t rsV = __builtin_amdgcn_make_buffer_rsrc((char*)vc + (size_t)(b * HKV + kvh) * SK * KVROW, 0, ABL_VSZ(SK * KVROW), 0x00020000);
  unsigned dummy = 0;
#define SP() spacer(dummy, rsZ)
#define DMAK(P, slot, so)                                                                        \
  do {                                                                                           \
    if (((P) & 3) == 0) dma0<0>((slot) + ((P) >> 2) * 4224, kv0, rsK, (so) + ((P) >> 2) * 4096); \
    else dmaN<((P) & 3) * 1056>(kv0, rsK, (so) + ((P) >> 2) * 4096 - ((P) & 3) * 32);            \
  } while (0)

  bf16x8 kf[8];
  unsigned kv0, kra;
  {
    const rsrc_t rsX = __builtin_amdgcn_make_buffer_rsrc((char*)x + (size_t)(b * SQ + ph * 80) * XROW, 0, 80 * XROW, 0x00020000);
    const rsrc_t rsWQ = __builtin_amdgcn_make_buffer_rsrc((char*)wq, 0, DM * WQROW, 0x00020000);
    const int lane = lane_now();
    unsigned wv[4];
    unit_voff<true>(wv, lane, WQROW);
    const unsigned xv = (lane >> 3) * XROW + ((unsigned)((lane & 7) ^ (lane >> 4) ^ ((wid & 1) << 2)) << 4);
    const unsigned rot = (unsigned)(2 * b + ph) % 7u;
    auto chunk = [&](int s) INL { const unsigned c = rot + s; return c >= 7 ? c - 7 : c; };
    constexpr unsigned XS[7] = {L_K0, L_K0 + 10240, L_K0 + 20480, L_P0, L_P1, L_MS, L_P0};
    auto aslot = [&](int s) INL { return s % 3 == 0 ? vrb : s % 3 == 1 ? vra : ks1; };
    const unsigned wso = hf * 32 * WQROW + h * 512 + w * 128;
    auto req_x = [&](int s) INL {
      dma0<0>(XS[s] + wid * 1024, xv, rsX, wid * 8 * XROW + chunk(s) * 128);
      if (wid < 2) dma0<0>(XS[s] + (wid + 8) * 1024, xv, rsX, (wid + 8) * 8 * XROW + chunk(s) * 128);
    };
    TA(10);
    if (TOUCH == 2 && wid == 3) {
      const rsrc_t rsW = __builtin_amdgcn_make_buffer_rsrc(ws, 0, WS_SLAB + 256 * SLAB, 0x00020000);
      const int l = lane_now(), grp = b * 2 + ph;
      const unsigned o = l < 8 ? WS_FLAG + (grp * 8 + l) * 128 : l < 16 ? WS_EPOCH + (grp * 8 + l - 8) * 128 : l < 32 ? WS_SLAB + (grp * 16 + l - 16) * SLAB : 0x80000000u;
      asm volatile("buffer_load_dword %0, %1, %2, 0 offen sc1" : "+{v255}"(dummy) : "v"(o), "s"(rsW) : "memory");
    }
    req_x(0); dma_unit(aslot(0) + hf * 4096, wv, rsWQ, chunk(0) * 64 * WQROW + wso);
    asm volatile("s_barrier" ::: "memory");
    req_x(1); dma_unit(aslot(1) + hf * 4096, wv, rsWQ, chunk(1) * 64 * WQROW + wso);
    TA(1);
    kv0 = (lane >> 5) * 512 + (((lane & 31) ^ (lane >> 5)) << 4);
    kra = ks0 + ((lane & 15) >> 1) * 1056 + (lane & 1) * 512 + (((lane >> 4) ^ (lane & 1)) << 4);
    unsigned tv[2], tk[2], xk[2], xp[2];
#pragma unroll
    for (int i = 0; i < 2; ++i) {
      const unsigned o = off_tr<true>(lane, 2 * hf + i);
      tv[i] = vra + o; tk[i] = ks1 + o; xk[i] = off_x(lane) ^ (64 * i); xp[i] = L_P0 + xk[i];
      asm volatile("" : "+v"(tv[i]), "+v"(tk[i]), "+v"(xk[i]), "+v"(xp[i]));
    }
    f32x4 acc[2][5];
    bf16x8 a0[2], b0[5], a1[2], b1[5];
    auto stepA = [&](int mma, int RS, int RH, int DS, int DH, bf16x8 (&ca)[2], bf16x8 (&cb)[5], bf16x8 (&na)[2], bf16x8 (&nb)[5]) INL {
#pragma unroll
      for (int i = 0; i < 10; ++i) {
        if (mma == 1) mfma_acc(acc[i & 1][i >> 1], ca[i & 1], cb[i >> 1]);
        if (mma == 2) mfma_init(acc[i & 1][i >> 1], ca[i & 1], cb[i >> 1]);
        if (RS >= 0) {
          const unsigned xa = RS < 3 ? xk[RH] + XS[RS] : xp[RH] + (XS[RS] - L_P0);
          if (i == 0 || i == 2) na[i >> 1] = ldfrag<true>((RS % 3 == 2 ? tk[i >> 1] : tv[i >> 1] + (RS % 3 == 0 ? 8192 : 0)) + RH * 4096);
          if (i == 1) nb[0] = lds128(xa);
          if (i >= 3 && i <= 6) nb[i - 2] = lds128(xa + (i - 2) * 2048);
        }
        if (DS >= 0) {
          const unsigned so = chunk(DS) * 64 * WQROW + wso;
          if (i == 7) { if (DH == 0) dma0<0>(aslot(DS) + hf * 4096, wv[0], rsWQ, so); else dmaN<2048>(wv[2], rsWQ, so); }
          if (i == 9) { if (DH == 0) dmaN<1024>(wv[1], rsWQ, so); else dmaN<3072>(wv[3], rsWQ, so); }
        }
        SBAR();
      }
    };
    if (wid < 2) WAITVM(6); else WAITVM(5);
    BARRIER();
    TA(2);
    STAMP(1);
    TB(0);
    stepA(false, 0, 0, -1, 0, a1, b1, a0, b0);
    {
#pragma unroll
      for (int i = 0; i < 5; ++i) {
        const unsigned p = wid + 8 * i, cs = p / 10, j = p - cs * 10;
        const unsigned sl = cs == 0 ? XS[2] : L_P0 + (cs - 1) * 10240;
        unsigned c = rot + 2 + cs; c = c >= 7 ? c - 7 : c;
        dma0<0>(sl + j * 1024, xv, rsX, j * 8 * XROW + c * 128);
      }
      const unsigned so = chunk(2) * 64 * WQROW + wso;
      dma0<0>(aslot(2) + hf * 4096, wv[0], rsWQ, so); dmaN<1024>(wv[1], rsWQ, so);
      SBAR();
    }
    TB(1);
    stepA(2, 0, 1, 2, 1, a0, b0, a1, b1);
    TB(2);
    WAITVM(9);
    BARRIER();
    TB(3);
#pragma unroll
    for (int s = 1; s < 5; ++s) {
      if (s == 4) { req_x(6); SBAR(); }
      stepA(true, s, 0, s + 2, 0, a1, b1, a0, b0);
      if (s == 3) TB(10);
      stepA(true, s, 1, s + 2, 1, a0, b0, a1, b1);
      if (s == 1) TB(4);
      if (s == 2) TB(7);
      if (s == 3) TB(11);
      WAITVM(4);
      if (s == 1) TB(5);
      if (s == 2) TB(8);
      if (s == 3) TB(12);
      BARRIER();
      if (s == 1) TB(6);
      if (s == 2) TB(9);
      if (s == 3) TB(13);
      if (s == 4) TB(14);
    }
    TA(3);
    if (hf == 0) {
      const unsigned so = TILE(0) * 32768 + w * 8192 + 128;
      DMAK(0, ks0, so); DMAK(1, ks0, so); DMAK(2, ks0, so); DMAK(3, ks0, so);
      DMAK(4, ks0, so); DMAK(5, ks0, so); DMAK(6, ks0, so); DMAK(7, ks0, so);
    } else {
      unit_voff<false>(wv, lane, KVROW);
      dma_unit(vra, wv, rsV, TILE(0) * 32768 + w * 128);
      dma_unit(vra + 4096, wv, rsV, TILE(0) * 32768 + 16384 + w * 128);
    }
    SBAR();
    stepA(true, 5, 0, -1, 0, a1, b1, a0, b0);
    stepA(true, 5, 1, -1, 0, a0, b0, a1, b1);
    WAITVM(8);
    BARRIER();
    stepA(true, 6, 0, -1, 0, a1, b1, a0, b0);
    stepA(true, 6, 1, -1, 0, a0, b0, a1, b1);
    if (hf == 0) {
      WAITVM(0);
#pragma unroll
      for (int ks = 0; ks < 8; ++ks) kf[ks] = lds128(kra + ks * 64);
      SBAR();
    }
    stepA(true, -1, 0, -1, 0, a1, b1, a0, b0);
    TA(4);
    MFMA_DRAIN();
    const int ksq = 2 * w + hf, g = lane >> 4;
    const unsigned qa = (ksq < 6 ? L_K1 + ksq * 5120 : L_P1 + (ksq - 6) * 5120) + ((g >> 1) * 16 + (lane & 15)) * 16 + (g & 1) * 8;
#pragma unroll
    for (int rt = 0; rt < 5; ++rt)
#pragma unroll
      for (int u = 0; u < 2; ++u) {
        const f32x4 v = acc[u][rt] * QSCALE;
        *(LAS u32x2*)(qa + rt * 1024 + u * 512) = u32x2{cvtpk(v[0], v[1]), cvtpk(v[2], v[3])};
        SBAR();
      }
    if (wid == 4 && lane < 32) *(LAS unsigned*)(L_FLAG + lane * 4) = 0u;
    WAITLGKM0();
    TA(11);
    if (hf == 0) {
      const unsigned so = TILE(1) * 32768 + w * 8192 + 128;
      DMAK(0, ks0, so); DMAK(1, ks0, so); SP(); DMAK(2, ks0, so); DMAK(3, ks0, so); SP();
      DMAK(4, ks0, so); DMAK(5, ks0, so); SP(); DMAK(6, ks0, so); DMAK(7, ks0, so); SP();
    }
    TA(5);
    asm volatile("s_barrier" ::: "memory");
    TBSAVE(true);
    TA(6);
    STAMP(2);
  }

  const unsigned rotC = (unsigned)(2 * b + ph) & 3u;
  const rsrc_t rsWO = __builtin_amdgcn_make_buffer_rsrc((char*)wo + (size_t)h * DH * WOROW, 0, wid == 7 ? 0 : DH * WOROW, 0x00020000);
  unsigned long long epoch;
  gu64* const epw = (gu64*)(ws + WS_EPOCH + ((b * 2 + ph) * 8 + wid) * 128);
  const rsrc_t rsS = __builtin_amdgcn_make_buffer_rsrc(ws + WS_SLAB + (size_t)((b * 2 + ph) * 16 + h) * SLAB, 0, SLAB, 0x00020000);
  auto o_bases = [&](unsigned (&ob)[4], int lane) INL {
#pragma unroll
    for (int i = 0; i < 4; ++i) { const unsigned cc = (rotC + i) & 3; ob[i] = (cc == 3 ? L_OI3 : L_P0 + cc * 10240) + lane * 16; }
  };

  if (wid < 4) {
    unsigned pwa;
    bf16x8 qf[5][8];
    {
      const int lane = lane_now();
      pwa = L_P0 + (w >> 1) * 5120 + (PLAY == 0 ? (w & 1) * 512 + lane * 8 : lane * 16 + (w & 1) * 8);
      unsigned q0 = L_K1 + lane * 16, q1 = L_P1 + lane * 16;
      asm volatile("" : "+v"(q0), "+v"(q1));
#pragma unroll
      for (int rt = 0; rt < 5; ++rt)
#pragma unroll
        for (int ks = 0; ks < 8; ++ks) qf[rt][ks] = lds128((ks < 6 ? q0 + ks * 5120 : q1 + (ks - 6) * 5120) + rt * 1024);
      SBAR();
    }
    float lsum[5] = {0.f, 0.f, 0.f, 0.f, 0.f}, tsr[5] = {0.f, 0.f, 0.f, 0.f, 0.f};
    f32x4 S0 = {0.f, 0.f, 0.f, 0.f};
#pragma unroll
    for (int ks = 0; ks < 8; ++ks) S0 = mfma(kf[ks], qf[0][ks], S0);
    TA(7);
    unsigned dslot = ks1;
    const unsigned pwsum = 2 * pwa + 10240, krsum = 2 * kra + L_K1;
    auto qk_tile = [&](int t, auto wo_c) INL -> unsigned {
      constexpr bool WO = decltype(wo_c)::value;
      unsigned so, wv[4];
      if constexpr (WO) { unit_voff<false>(wv, lane_now(), WOROW); so = ((rotC + t - 14) & 3) * 64 * WOROW + w * 128; }
      else so = TILE(t + 2) * 32768 + w * 8192 + 128;
      unsigned fa = L_FLAG + t * 4;
      asm volatile("" : "+v"(fa));
      asm volatile("" : "+s"(so));
      TR(11);
      BARRIER();
      TR(12);
      TRSAVE(t == TRT + 1);
      if (t == 1) { STAMP(3); cycB = __builtin_amdgcn_s_memtime(); }
      if (t == 13) { STAMP(4); stamp[10] = stamp[0] + (__builtin_amdgcn_s_memtime() - cycB) / 120; }
      if (t == 0) TA(8);
      if (t == 1) { TA(9); TASAVE(true); }
      unsigned fl;
      f32x4 S1;
      float p0, p1, p2, p3, tm;
      u32x2 pk;
#define QDMA(P)                                                                                                   \
  do {                                                                                                            \
    if constexpr (WO) {                                                                                           \
      if (((P) & 3) == 0) dma0<0>(dslot + ((P) >> 2) * 4096, wv[0], rsWO, so + ((P) >> 2) * 32 * WOROW);           \
      else dmaN<((P) & 3) * 1024>(wv[(P) & 3], rsWO, so + ((P) >> 2) * 32 * WOROW);                                \
    } else DMAK(P, dslot, so);                                                                                    \
  } while (0)
#define QM(i, RN, SN) if ((i) == 0) mfma_init(SN, kf[i], qf[RN][i]); else mfma_acc(SN, kf[i], qf[RN][i]);
#define QKBURST(RC, SC, RN, SN)                                                                                    \
  TR(2 * RC); QM(0, RN, SN) SBAR(); PRIO_HI(); p0 = ex2(SC[0]); SBAR();                                            \
  QM(1, RN, SN) SBAR(); p1 = ex2(SC[1]); SBAR();                                                                   \
  QM(2, RN, SN) SBAR(); p2 = ex2(SC[2]); SBAR();                                                                   \
  QM(3, RN, SN) SBAR(); p3 = ex2(SC[3]); SBAR();                                                                   \
  QM(4, RN, SN) SBAR(); pk[0] = cvtpk(p0, p1); p0 = fadd_s(p0, p1); SBAR();                                        \
  QM(5, RN, SN) SBAR(); pk[1] = cvtpk(p2, p3); p2 = fadd_s(p2, p3); SBAR();                                        \
  QM(6, RN, SN) SBAR(); lsum[RC] = fadd_s(lsum[RC], tsr[RC]); tsr[RC] = fadd_s(p0, p2); SBAR();                    \
  QM(7, RN, SN) SBAR(); PRIO_LO(); TR(2 * RC + 1);
#define QKFIN(RC)                                                                                                  \
  *(LAS u32x2*)(pwa + RC * 1024) = pk;                                                                             \
  tm = RC == 0 ? tsr[0] : fadd_s(tm, tsr[RC]); SBAR();
      QKBURST(0, S0, 1, S1)
      fl = *(LAS unsigned*)fa;
      QKFIN(0)
      QDMA(0); QDMA(1); SP(); SBAR();
      QKBURST(1, S1, 2, S0)
      QKFIN(1)
      unsigned long long bad = __ballot(fl != 0);
      QDMA(2); QDMA(3); SP(); SBAR();
      QKBURST(2, S0, 3, S1)
      QKFIN(2)
      QDMA(4); QDMA(5); SP(); SBAR();
      QKBURST(3, S1, 4, S0)
      WAITVM(10);
#pragma unroll
      for (int ks = 0; ks < 8; ++ks) kf[ks] = lds128(kra + ks * 64);
      kra = krsum - kra;
      asm volatile("" : "+v"(kra));
      SBAR();
      QKFIN(3)
      QKBURST(4, S0, 0, S1)
      QKFIN(4)
      bad |= __ballot(!(tm <= PTHR));
      pwa = pwsum - pwa;
      asm volatile("" : "+v"(pwa));
      SBAR();
      QDMA(6); QDMA(7); SP(); SBAR();
      dslot = ks0 + ks1 - dslot;
      S0 = S1;
#undef QKFIN
#undef QKBURST
#undef QM
#undef QDMA
      if (!WO && __builtin_expect(t == 0, 0)) bad |= __ballot(!(fminf(fminf(fminf(tsr[0], tsr[1]), fminf(tsr[2], tsr[3])), tsr[4]) >= PLOW));
      TR(10);
      return (unsigned)bad | (unsigned)(bad >> 32);
    };
    int t = -1;
    unsigned abn, last = 0x4000u;
#pragma unroll 1
    do { ++t; last >>= 1; asm volatile("" : "+s"(last)); abn = qk_tile(t, std::false_type{}); } while ((abn | (last & 1u)) == 0);
    if (abn == 0) {
#pragma unroll 1
      do { ++t; abn = qk_tile(t, std::true_type{}); } while ((abn | (t & 1)) == 0);
    }
    int entry = 2;
    if (abn == 0) {
#pragma unroll
      for (int rt = 0; rt < 5; ++rt) lsum[rt] += tsr[rt];
      t = 16;
      BARRIER();
      entry = __builtin_amdgcn_readfirstlane(*(LAS unsigned*)(L_FLAG + 64)) != 0;
    } else {
      kra = krsum - kra;
      pwa = pwsum - pwa;
    }
    if (__builtin_expect(entry != 0, 0)) {
      MFMA_DRAIN();
      float negm[5] = {0.f, 0.f, 0.f, 0.f, 0.f};
#pragma unroll 1
      for (;; ++t) {
        if (entry == 0) BARRIER();
        {
          const unsigned fl = __builtin_amdgcn_readfirstlane(*(LAS unsigned*)(L_FLAG + t * 4));
          if (fl != 0) {
            const unsigned pprev = pwsum - pwa;
            const int row = lane_now() & 15;
#pragma unroll
            for (int rt = 0; rt < 5; ++rt) {
              const float D = read_D(rt, fl, row);
              const float own = ((fl >> (8 * w)) & 1) ? ldsf(L_DLT + (w * 80 + rt * 16 + row) * 4) : 0.f;
              const float f = ex2(own - D);
              const u32x2 pk = *(LAS u32x2*)(pprev + rt * 1024);
              const float a0 = __uint_as_float(pk[0] << 16) * f, a1 = __uint_as_float(pk[0] & 0xffff0000u) * f;
              const float a2 = __uint_as_float(pk[1] << 16) * f, a3 = __uint_as_float(pk[1] & 0xffff0000u) * f;
              *(LAS u32x2*)(pprev + rt * 1024) = u32x2{cvtpk(a0, a1), cvtpk(a2, a3)};
              lsum[rt] *= f;
              negm[rt] -= D;
              SBAR();
            }
            BARRIER();
          }
        }
        if (t == 16) break;
        if (entry != 2) {
          const unsigned dslot = (t & 1) ? ks0 : ks1;
          if (t < 14) {
            const unsigned so = TILE(t + 2) * 32768 + w * 8192 + 128;
            DMAK(0, dslot, so); DMAK(1, dslot, so); DMAK(2, dslot, so); DMAK(3, dslot, so);
            DMAK(4, dslot, so); DMAK(5, dslot, so); DMAK(6, dslot, so); DMAK(7, dslot, so);
          } else {
            unsigned wv[4];
            unit_voff<false>(wv, lane_now(), WOROW);
            const unsigned so = ((rotC + t - 14) & 3) * 64 * WOROW + w * 128;
            dma_unit(dslot, wv, rsWO, so);
            dma_unit(dslot + 4096, wv, rsWO, so + 32 * WOROW);
          }
        } else {
          const int lane = lane_now();
#pragma unroll
          for (int ks = 0; ks < 8; ++ks)
            kf[ks] = __builtin_bit_cast(bf16x8, __builtin_amdgcn_raw_buffer_load_b128(rsK, (lane & 15) * 512 + (lane >> 4) * 16 + ks * 64, TILE(t) * 32768 + w * 8192 + 128, 0));
        }
        f32x4 S[5];
        float mx[5];
        bool need = false;
#pragma unroll
        for (int rt = 0; rt < 5; ++rt) {
          S[rt] = f32x4{negm[rt], negm[rt], negm[rt], negm[rt]};
#pragma unroll
          for (int ks = 0; ks < 8; ++ks) S[rt] = mfma(kf[ks], qf[rt][ks], S[rt]);
          mx[rt] = ceilf(xmax4(fmaxf(fmaxf(S[rt][0], S[rt][1]), fmaxf(S[rt][2], S[rt][3]))));
          need |= !(mx[rt] <= GTHR) || (t == 0 && !(mx[rt] >= -GTHR));
          SBAR();
        }
        need = __any(need);
        const int lane = lane_now();
#pragma unroll
        for (int rt = 0; rt < 5; ++rt) {
          const float dl = need ? (t > 0 ? fmaxf(mx[rt], 0.f) : mx[rt]) : 0.f;
          const float p0 = ex2(S[rt][0] - dl), p1 = ex2(S[rt][1] - dl), p2 = ex2(S[rt][2] - dl), p3 = ex2(S[rt][3] - dl);
          const float ts = (p0 + p1) + (p2 + p3);
          lsum[rt] = t > 0 ? lsum[rt] * ex2(-dl) + ts : ts;
          *(LAS u32x2*)(pwa + rt * 1024) = u32x2{cvtpk(p0, p1), cvtpk(p2, p3)};
          if (need && lane < 16) *(LAS float*)(L_DLT + (w * 80 + rt * 16 + lane) * 4) = dl;
          SBAR();
        }
        if (need) *(LAS unsigned char*)(L_FLAG + (t + 1) * 4 + w) = 1;
        WAITVM(8);
#pragma unroll
        for (int ks = 0; ks < 8; ++ks) kf[ks] = lds128(kra + ks * 64);
        kra = krsum - kra;
        pwa = pwsum - pwa;
        entry = 0;
      }
    }
    {
      const int lane = lane_now();
#pragma unroll
      for (int rt = 0; rt < 5; ++rt) {
        const float l = xsum4(lsum[rt]);
        if (lane < 16) *(LAS float*)ls_addr(w, rt, lane) = l;
      }
    }
    const int lane = lane_now();
    unsigned t1[4], wv[4], ob[4];
#pragma unroll
    for (int dt = 0; dt < 4; ++dt) t1[dt] = ks0 + off_tr<false>(lane, dt);
    unit_voff<false>(wv, lane, WOROW);
    o_bases(ob, lane);
    bf16x8 af[8][4];
    const unsigned so2 = ((rotC + 2) & 3) * 64 * WOROW, so3 = ((rotC + 3) & 3) * 64 * WOROW;
    WAITVM(8);
#pragma unroll
    for (int j = 0; j < 2; ++j)
#pragma unroll
      for (int dt = 0; dt < 4; ++dt) af[j][dt] = ldfrag<false>(t1[dt] + L_K1 + j * 4096);
    WAITLGKM0();
    dma_unit(ks1, wv, rsWO, so2 + w * 128); SP();
    dma_unit(ks1 + 4096, wv, rsWO, so2 + 32 * WOROW + w * 128); SP();
    WAITVM(10);
#pragma unroll
    for (int j = 2; j < 4; ++j)
#pragma unroll
      for (int dt = 0; dt < 4; ++dt) af[j][dt] = ldfrag<false>(t1[dt] + (j & 1) * 4096);
    BARRIER();
    STAMP(5);
    dma_unit(ks0, wv, rsWO, so3 + w * 128); SP();
    dma_unit(ks0 + 4096, wv, rsWO, so3 + 32 * WOROW + w * 128); SP();
#pragma unroll
    for (int j = 4; j < 8; ++j) {
      if (w < 3) WAITVM(16); else if (j == 4) WAITVM(16); else if (j == 5) WAITVM(11); else if (j == 6) WAITVM(6); else WAITVM(1);
#pragma unroll
      for (int dt = 0; dt < 4; ++dt) af[j][dt] = ldfrag<false>(t1[dt] + (j < 6 ? L_K1 : 0) + (j & 1) * 4096);
      WAITLGKM0();
      if (w < 3) { dma_unit((j < 6 ? ks1 : ks0) + (j & 1) * 4096, wv, rsWO, (j < 6 ? so2 : so3) + (j & 1) * 32 * WOROW + (4 + w) * 128); SP(); }
    }
    asm volatile("s_barrier" ::: "memory");
    STAMP(6);
    oproj<4>(af, ob, rsS, w * 10240 + lane * 16, 0, [&]() INL {
      WAITVM(0);
      asm volatile("s_barrier" ::: "memory");
      epoch = __hip_atomic_load(epw + (lane >> 6), __ATOMIC_RELAXED, __HIP_MEMORY_SCOPE_AGENT);
    });
    STAMP(7);
  } else {
    const int lane = lane_now(), row = lane & 15;
    unsigned ta[4], dv[4];
#pragma unroll
    for (int dt = 0; dt < 4; ++dt) ta[dt] = vra + off_tr<false>(lane, dt);
    unit_voff<false>(dv, lane, KVROW);
    if (TOUCH == 1 && wid == 7) {
      const rsrc_t rsW = __builtin_amdgcn_make_buffer_rsrc(ws, 0, WS_SLAB + 256 * SLAB, 0x00020000);
      const int l = lane_now(), grp = b * 2 + ph;
      const unsigned o = l < 8 ? WS_FLAG + (grp * 8 + l) * 128 : l < 16 ? WS_EPOCH + (grp * 8 + l - 8) * 128 : l < 32 ? WS_SLAB + (grp * 16 + l - 16) * SLAB : 0x80000000u;
      asm volatile("buffer_load_dword %0, %1, %2, 0 offen sc1" : "+{v255}"(dummy) : "v"(o), "s"(rsW) : "memory");
    }
    dma_unit(vrb, dv, rsV, TILE(1) * 32768 + w * 128);
    SP();
    dma_unit(vrb + 4096, dv, rsV, TILE(1) * 32768 + 16384 + w * 128);
    SP();
    f32x4 acc[4][5];
    zero_acc(acc);
    bf16x8 va[2][4], vb[2][4];
    WAITVM(10);
#pragma unroll
    for (int hh = 0; hh < 2; ++hh)
#pragma unroll
      for (int dt = 0; dt < 4; ++dt) va[hh][dt] = ldfrag<false>(ta[dt] + hh * 4096);
    unsigned pa = L_P0 + lane * (PLAY == 0 ? 8 : 16);
    asm volatile("" : "+v"(pa));
    TA(7);
    BARRIER();
    TA(8);
    TASAVE(true);
    rsrc_t rsD = rsV;
    unsigned dstep = 16384;
    auto pv_tile = [&](int t, auto par_c, bf16x8 (&vc)[2][4], bf16x8 (&vn)[2][4]) INL {
      constexpr int PAR = decltype(par_c)::value;
      unsigned so;
      if (t < 14) so = TILE(t + 2) * 32768 + w * 128;
      else so = ((rotC + t - 14) & 3) * 64 * WOROW + wid * 128;
      const unsigned dsl = vra + PAR * 8192;
      unsigned fa = L_FLAG + (t + 1) * 4;
      asm volatile("" : "+v"(fa));
      asm volatile("" : "+s"(so));
      bf16x8 pf[5][2];
#define RDP(rt) { pf[rt][0] = ldp(pa + PAR * 10240 + (rt) * 1024); pf[rt][1] = ldp(pa + PAR * 10240 + 5120 + (rt) * 1024); }
#define PM(i, rt) mfma_acc(acc[(i) & 3][rt], vc[(i) >> 2][(i) & 3], pf[rt][(i) >> 2]);
#define RDV(dt, hh) vn[hh][dt] = ldfrag<false>(ta[dt] + (PAR ^ 1) * 8192 + (hh) * 4096);
#define BURST(rt)                                                                                                  \
  SBAR(); TR(2 * rt + 1); PM(0, rt) SBAR(); PRIO_HI(); SBAR(); PM(1, rt) PM(2, rt) PM(3, rt) PM(4, rt) PM(5, rt) PM(6, rt) PM(7, rt) SBAR(); PRIO_LO(); TR(2 * rt + 2); SBAR();
#define PD(P)                                                                                                      \
  {                                                                                                                \
    if (((P) & 3) == 0) dma0<0>(dsl + ((P) >> 2) * 4096, dv[0], rsD, (P) ? so + dstep : so);                       \
    else dmaN<((P) & 3) * 1024>(dv[(P) & 3], rsD, (P) >> 2 ? so + dstep : so);                                     \
  }
      TR(11);
      BARRIER();
      TR(12);
      TRSAVE(t == TRT);
      TR(0);
      unsigned fl = *(LAS unsigned*)fa;
      RDP(0) RDP(1)
      SBAR();
      PD(0) SP();
      SBAR();
      fl = __builtin_amdgcn_readfirstlane(fl);
      if (__builtin_expect(fl != 0, 0)) {
        if (t > 0) {
#pragma unroll
          for (int rt = 0; rt < 5; ++rt) {
            const float f = ex2(-read_D(rt, fl, row));
#pragma unroll
            for (int dt = 0; dt < 4; ++dt) acc[dt][rt] *= f;
          }
        }
        BARRIER();
        RDP(0) RDP(1)
      }
      BURST(0)
      RDP(2) SBAR();
      PD(1) PD(2) SP();
      BURST(1)
      RDP(3) SBAR();
      WAITVM(11);
      RDV(0, 0) RDV(1, 0) SBAR();
      PD(3)
      BURST(2)
      RDP(4) RDV(2, 0) RDV(3, 0) SBAR();
      PD(4) PD(5) SP();
      if (PAR == 1 && t == 15) BARRIER();
      BURST(3)
      WAITVM(10);
      RDV(0, 1) RDV(1, 1) RDV(2, 1) RDV(3, 1) SBAR();
      PD(6) PD(7) SP();
      BURST(4)
#undef PD
#undef BURST
#undef RDV
#undef PM
#undef RDP
    };
#pragma unroll 1
    for (int t = 0; t < 16; t += 2) {
      if (t == 14) { unit_voff<false>(dv, lane_now(), WOROW); rsD = rsWO; dstep = 32 * WOROW; }
      pv_tile(t, std::integral_constant<int, 0>{}, va, vb);
      pv_tile(t + 1, std::integral_constant<int, 1>{}, vb, va);
    }
    STAMP(5);
    float inv[5];
#pragma unroll
    for (int rt = 0; rt < 5; ++rt) {
      float l = 0.f;
#pragma unroll
      for (int ww = 0; ww < 4; ++ww) l += ldsf(ls_addr(ww, rt, row));
      inv[rt] = __builtin_amdgcn_rcpf(l);
    }
    if (w == 3) WAITVM(0);
    MFMA_DRAIN();
    SBAR();
    const unsigned oa = (w == 3 ? L_OI3 : L_P0 + w * 10240) + lane * 16;
#pragma unroll
    for (int rt = 0; rt < 5; ++rt)
#pragma unroll
      for (int ks = 0; ks < 2; ++ks) {
        const f32x4 u = acc[2 * ks][rt] * inv[rt], v = acc[2 * ks + 1][rt] * inv[rt];
        *(LAS u32x4*)(oa + ks * 5120 + rt * 1024) = u32x4{cvtpk(u[0], u[1]), cvtpk(u[2], u[3]), cvtpk(v[0], v[1]), cvtpk(v[2], v[3])};
        SBAR();
      }
    unsigned ob[4], tA[3], tB[3];
    o_bases(ob, lane);
#pragma unroll
    for (int i = 0; i < 3; ++i) {
      const int T = 3 * w + i, o = T >> 2;
      const unsigned tr = off_tr<false>(lane, T & 3);
      tA[i] = L_VR + o * 16384 + tr; tB[i] = L_K0 + o * KSLOT + tr;
    }
    WAITVM(0);
    BARRIER();
    STAMP(6);
    bf16x8 af[8][3];
#pragma unroll
    for (int j = 0; j < 4; ++j)
#pragma unroll
      for (int i = 0; i < 3; ++i) af[j][i] = ldfrag<false>(tA[i] + j * 4096);
    asm volatile("s_barrier" ::: "memory");
#pragma unroll
    for (int j = 4; j < 8; ++j)
#pragma unroll
      for (int i = 0; i < 3; ++i) af[j][i] = ldfrag<false>(tB[i] + (j < 6 ? L_K1 : 0) + (j & 1) * 4096);
    oproj<3>(af, ob, rsS, 40960 + w * 7680 + lane * 16, 40960 + w * 7680 + 7168 + lane * 8, [&]() INL { epoch = __hip_atomic_load(epw + (lane >> 6), __ATOMIC_RELAXED, __HIP_MEMORY_SCOPE_AGENT); });
    STAMP(7);
  }
  {
    unsigned long long tag = epoch + 0x9E3779B97F4A7C15ull;
    tag = (tag ^ (tag >> 30)) * 0xBF58476D1CE4E5B9ull;
    tag = (tag ^ (tag >> 27)) * 0x94D049BB133111EBull;
    tag ^= tag >> 31;
    const int lane = lane_now();
    gu64* fl = (gu64*)(ws + WS_FLAG + ((b * 2 + ph) * 8 + wid) * 128);
    const int nch = hf ? 30 : 40, r = h * nch + (lane < nch ? lane : 0), piece = r >> 6;
    int nt0, nt1, rt0, rt1, l0 = r & 63, l1 = r & 63;
    if (hf == 0) {
      nt0 = 4 * w + 2 * (piece & 1); nt1 = nt0 + 1; rt0 = rt1 = piece >> 1;
    } else {
      nt0 = 16 + 3 * w + (piece < 5 ? 0 : 2); nt1 = 16 + 3 * w + (piece < 5 ? 1 : 2);
      rt0 = piece < 5 ? piece : 2 * (piece - 5); rt1 = piece < 5 ? piece : piece < 7 ? 2 * (piece - 5) + 1 : 4;
      if (piece == 7) { l0 = 2 * l0; l1 = l0 + 1; }
    }
    const unsigned d0 = ((16 * rt0 + (l0 & 15)) * DM + 16 * nt0 + 4 * (l0 >> 4)) * 2, d1 = ((16 * rt1 + (l1 & 15)) * DM + 16 * nt1 + 4 * (l1 >> 4)) * 2;
    const unsigned src = lane < nch ? (hf ? 40960 + w * 7680 : w * 10240) + r * 16 : 0x80000000u;
    const rsrc_t rsG = __builtin_amdgcn_make_buffer_rsrc(ws + WS_SLAB + (size_t)(b * 2 + ph) * 16 * SLAB, 0, 16 * SLAB, 0x00020000);
    unsigned long long f = ~tag;
    if (hf == 0) f = __hip_atomic_load(fl + (lane & 15), __ATOMIC_RELAXED, __HIP_MEMORY_SCOPE_AGENT);
    WAITVM(0);
    asm volatile("" :: "{v255}"(dummy));
    STAMP(12);
    if (lane == 0) __hip_atomic_store(fl + h, tag, __ATOMIC_RELAXED, __HIP_MEMORY_SCOPE_AGENT);
    u32x4 v[16];
#pragma unroll
    for (int hh = 0; hh < 16; ++hh) v[hh] = u32x4{0u, 0u, 0u, 0u};
    unsigned pend = 0xffffu;
#pragma unroll 1
    for (unsigned spin = 0; spin < (1u << 22); ++spin) {
      const unsigned rdy = ((unsigned)__ballot(f == tag) | (1u << h)) & pend;
      __builtin_amdgcn_fence(__ATOMIC_ACQUIRE, "wavefront");
      asm volatile("" ::: "memory");
#pragma unroll
      for (int hh = 0; hh < 16; ++hh)
        if ((rdy >> hh) & 1) v[hh] = __builtin_amdgcn_raw_buffer_load_b128(rsG, src, hh * SLAB, 16);
      pend &= ~rdy;
      if (pend == 0) break;
      if (rdy == 0) __builtin_amdgcn_s_sleep(SLP);
      f = __hip_atomic_load(fl + (lane & 15), __ATOMIC_RELAXED, __HIP_MEMORY_SCOPE_AGENT);
    }
    STAMP(13);
    if (lane < nch) {
      if (h == 0 && lane == 0) __hip_atomic_store(epw, epoch + 1, __ATOMIC_RELAXED, __HIP_MEMORY_SCOPE_AGENT);
      float sm[8] = {0.f, 0.f, 0.f, 0.f, 0.f, 0.f, 0.f, 0.f};
#pragma unroll
      for (int hh = 0; hh < 16; ++hh)
#pragma unroll
        for (int i = 0; i < 4; ++i) { sm[2 * i] += __uint_as_float(v[hh][i] << 16); sm[2 * i + 1] += __uint_as_float(v[hh][i] & 0xffff0000u); }
      char* dst = out + (size_t)(b * 2 + ph) * 80 * DM * 2;
      *(u32x2*)(dst + d0) = u32x2{cvtpk(sm[0], sm[1]), cvtpk(sm[2], sm[3])};
      *(u32x2*)(dst + d1) = u32x2{cvtpk(sm[4], sm[5]), cvtpk(sm[6], sm[7])};
    }
  }
  WAITVM(0);
  STAMP(8);
  if (wid == 0) {
    unsigned long long d = stamp[4] - stamp[3];
    while (__builtin_amdgcn_s_memrealtime() - stamp[0] < 6000 + d) __builtin_amdgcn_s_sleep(1);
  }
}

extern "C" void kernel_launch(void* const* d_in, const int* in_sizes, int n_in, void* d_out, int out_size, void* d_ws, size_t ws_size,
                              hipStream_t stream) {
  (void)in_sizes; (void)n_in; (void)out_size; (void)ws_size;
  (void)hipFuncSetAttribute((const void*)gqa_fused, hipFuncAttributeMaxDynamicSharedMemorySize, L_TOTAL);
  gqa_fused<<<256, 512, L_TOTAL, stream>>>(d_in[0], d_in[1], d_in[2], d_in[3], d_in[4], (char*)d_ws, (char*)d_out);
}
```

```cpp
#include <hip/hip_runtime.h>
#include <hip/hip_bf16.h>
#include <type_traits>
#define PP 1
#define SPT 1
#define SPCQ 1
#define SPCP 1
#define TRT 7
#define SLP 4
#define TOUCH 2
#define PLAY 0
#define CPF 1
#define ABL 0
#pragma clang diagnostic ignored "-Wint-to-pointer-cast"

typedef __bf16 bf16x8 __attribute__((ext_vector_type(8)));
typedef __bf16 bf16x4 __attribute__((ext_vector_type(4)));
typedef __bf16 bf16x2 __attribute__((ext_vector_type(2)));
typedef float f32x4 __attribute__((ext_vector_type(4)));
typedef float f32x2 __attribute__((ext_vector_type(2)));
typedef unsigned u32x4 __attribute__((ext_vector_type(4)));
typedef unsigned u32x2 __attribute__((ext_vector_type(2)));
#define LAS __attribute__((address_space(3)))
#define DI __device__ __forceinline__
typedef __amdgpu_buffer_rsrc_t rsrc_t;

constexpr int NB = 8, SQ = 160, DM = 448, HQ = 16, HKV = 8, SK = 1024, DH = 256;
constexpr unsigned XROW = DM * 2, WQROW = HQ * DH * 2, KVROW = DH * 2, WOROW = DM * 2;
constexpr unsigned SLAB = 80 * DM * 2;
constexpr unsigned WS_FLAG = 0, WS_EPOCH = 32768, WS_SLAB = 65536;
typedef __attribute__((address_space(1))) unsigned long long gu64;
constexpr unsigned KSLOT = 8448;
constexpr unsigned L_K0 = 0, L_K1 = 4 * KSLOT, L_VR = 8 * KSLOT, L_P0 = L_VR + 65536, L_P1 = L_P0 + 10240, L_MS = L_P1 + 10240, L_TOTAL = L_MS + 10240;
static_assert(L_TOTAL == 163840, "LDS");
constexpr unsigned L_FLAG = L_MS;
constexpr unsigned L_DLT = L_MS + 128;
constexpr unsigned L_OI3 = L_VR + 3 * 16384;
constexpr int CODE_BYTES = 24192;
constexpr float QSCALE = 0.0625f * 1.4426950408889634f;
#define PTHR 65536.f
#define GTHR 12.f
#define PLOW (1.f / 65536.f)

#define STAMP(k)
#define ABL_KSZ(x) ((ABL & 1) ? 0 : (x))
#define ABL_VSZ(x) ((ABL & 1024) ? 0 : (x))
#define SBAR() __builtin_amdgcn_sched_barrier(0)
#define TRX(k) do { SBAR(); asm volatile("s_memtime %0" : "=s"(tr[k])); SBAR(); } while (0)
#define TRXSAVE(cond) do { if (cond) { WAITLGKM0(); if (lane_now() == 0) for (int k_ = 0; k_ < 16; ++k_) ((unsigned long long*)(ws + (80u << 20)))[(bid * 8 + wid) * 16 + k_] = tr[k_]; } } while (0)
#define TR(k)
#define TRSAVE(cond)
#define TB(k)
#define TBSAVE(cond)
#define TA(k)
#define TASAVE(cond)
#define PRIO_HI() __builtin_amdgcn_s_setprio(1)
#define PRIO_LO() __builtin_amdgcn_s_setprio(0)
#define WAITVM(n) asm volatile("s_waitcnt vmcnt(%0)" ::"i"(n) : "memory")
#define WAITLGKM0() do { asm volatile("" ::: "memory"); __builtin_amdgcn_s_waitcnt(0xC07F); asm volatile("" ::: "memory"); } while (0)
#define BARRIER() do { WAITLGKM0(); asm volatile("s_barrier" ::: "memory"); } while (0)

DI int lane_now() { unsigned z = 0; asm volatile("" : "+v"(z)); return __builtin_amdgcn_mbcnt_hi(~0u, __builtin_amdgcn_mbcnt_lo(~0u, z)); }
DI bf16x8 lds128(unsigned a) { return *(LAS bf16x8*)a; }
DI bf16x4 ldstr(unsigned a) { return __builtin_amdgcn_ds_read_tr16_b64_v4bf16((LAS bf16x4*)a); }
DI float ldsf(unsigned a) { return *(LAS float*)a; }
DI unsigned cvtpk(float lo, float hi) { f32x2 v = {lo, hi}; return __builtin_bit_cast(unsigned, __builtin_convertvector(v, bf16x2)); }
DI f32x4 mfma(bf16x8 a, bf16x8 b, f32x4 c) { return __builtin_amdgcn_mfma_f32_16x16x32_bf16(a, b, c, 0, 0, 0); }
DI void mfma_acc(f32x4& c, bf16x8 a, bf16x8 b) { asm volatile("v_mfma_f32_16x16x32_bf16 %0, %1, %2, %0" : "+v"(c) : "v"(a), "v"(b)); }
DI void mfma_init(f32x4& d, bf16x8 a, bf16x8 b) { asm volatile("v_mfma_f32_16x16x32_bf16 %0, %1, %2, 0" : "=&v"(d) : "v"(a), "v"(b)); }
#define MFMA_DRAIN() asm volatile("s_nop 7\n\ts_nop 1" ::: "memory")
DI float ex2(float x) { return __builtin_amdgcn_exp2f(x); }
template <bool NAT> DI void unit_voff(unsigned (&v)[4], int lane, unsigned stride) {
  const unsigned e = (lane >> 3) * stride + ((unsigned)((lane & 7) ^ (((lane >> 4) & (NAT ? 1 : 3)) << 1)) << 4);
#pragma unroll
  for (int p = 0; p < 4; ++p) v[p] = ((NAT && (p & 1)) ? e ^ 64 : e) + p * (8 * stride - 1024);
}
template <bool NAT> DI unsigned off_tr(int lane, int dt) {
  const int g = lane >> 4, qq = (lane >> 2) & 3, pp = lane & 3, f = ((qq >> 1) & 1) | ((g & 1) << 1);
  return ((NAT ? 8 : 4) * g + qq) * 128 + ((dt ^ f) << 5) + pp * 8;
}
template <bool NAT> DI bf16x8 ldfrag(unsigned a) { return __builtin_shufflevector(ldstr(a), ldstr(a + (NAT ? 512 : 2048)), 0, 1, 2, 3, 4, 5, 6, 7); }
DI unsigned off_x(int lane) { return (lane & 15) * 128 + (((lane >> 4) ^ (((lane & 15) >> 1) & 7)) << 4); }
DI bf16x8 ldp(unsigned a) {
  const bf16x4 lo = *(LAS bf16x4*)a, hi = *(LAS bf16x4*)(a + 512);
  return __builtin_shufflevector(lo, hi, 0, 1, 2, 3, 4, 5, 6, 7);
}
DI float xmax4(float m) {
  auto a = __builtin_amdgcn_permlane16_swap(__float_as_uint(m), __float_as_uint(m), false, false);
  m = fmaxf(__uint_as_float(a[0]), __uint_as_float(a[1]));
  auto c = __builtin_amdgcn_permlane32_swap(__float_as_uint(m), __float_as_uint(m), false, false);
  return fmaxf(__uint_as_float(c[0]), __uint_as_float(c[1]));
}
DI float xsum4(float m) {
  auto a = __builtin_amdgcn_permlane16_swap(__float_as_uint(m), __float_as_uint(m), false, false);
  m = __uint_as_float(a[0]) + __uint_as_float(a[1]);
  auto c = __builtin_amdgcn_permlane32_swap(__float_as_uint(m), __float_as_uint(m), false, false);
  return __uint_as_float(c[0]) + __uint_as_float(c[1]);
}

template <int IMM> DI void dma0(unsigned ldsaddr, unsigned voff, rsrc_t rs, unsigned soff) {
  asm volatile("s_mov_b32 m0, %0\n\ts_nop 0\n\tbuffer_load_dwordx4 %1, %2, %3 offen offset:%4 lds" ::"s"(ldsaddr), "v"(voff), "s"(rs), "s"(soff), "i"(IMM) : "memory");
}
template <int IMM> DI void dmaN(unsigned voff, rsrc_t rs, unsigned soff) {
  asm volatile("buffer_load_dwordx4 %0, %1, %2 offen offset:%3 lds" ::"v"(voff), "s"(rs), "s"(soff), "i"(IMM) : "memory");
}
DI void spacer(unsigned& dummy, rsrc_t rz) { asm volatile("buffer_load_dword %0, off, %1, 0" : "+{v255}"(dummy) : "s"(rz) : "memory"); }
DI void dma_unit(unsigned ldsaddr, const unsigned (&v)[4], rsrc_t rs, unsigned soff) {
  dma0<0>(ldsaddr, v[0], rs, soff); dmaN<1024>(v[1], rs, soff); dmaN<2048>(v[2], rs, soff); dmaN<3072>(v[3], rs, soff);
}
template <int NT, class MID>
DI void oproj(const bf16x8 (&af)[8][NT], const unsigned (&ob)[4], rsrc_t rsS, unsigned sbase, unsigned shalf, MID mid) {
  bf16x8 bf[2][8];
  f32x4 acc[2][NT];
  u32x2 keep = {0u, 0u};
#pragma unroll
  for (int j = 0; j < 8; ++j) bf[0][j] = lds128(ob[j >> 1] + (j & 1) * 5120);
  SBAR();
#pragma unroll
  for (int rt = 0; rt <= 5; ++rt) {
    const int c = rt & 1, p = c ^ 1;
    u32x4 d;
    if (rt == 5) MFMA_DRAIN();
#pragma unroll
    for (int i = 0; i < 8 * NT; ++i) {
      const int j = i / NT, n = i % NT;
      if (rt < 5) { if (j == 0) mfma_init(acc[c][n], af[j][n], bf[c][j]); else mfma_acc(acc[c][n], af[j][n], bf[c][j]); }
      if (rt < 4 && n == 1) bf[p][j] = lds128(ob[j >> 1] + (j & 1) * 5120 + (rt + 1) * 1024);
      if (rt > 0) {
        const int r = rt - 1;
        if (i == 8) { d[0] = cvtpk(acc[p][0][0], acc[p][0][1]); d[1] = cvtpk(acc[p][0][2], acc[p][0][3]); }
        if (i == 9) { d[2] = cvtpk(acc[p][1][0], acc[p][1][1]); d[3] = cvtpk(acc[p][1][2], acc[p][1][3]); }
        if (i == 11) __builtin_amdgcn_raw_buffer_store_b128(d, rsS, sbase + (NT == 4 ? r * 2048 : r * 1024), 0, 16);
        if (NT == 4) {
          if (i == 14) { d[0] = cvtpk(acc[p][2][0], acc[p][2][1]); d[1] = cvtpk(acc[p][2][2], acc[p][2][3]); }
          if (i == 15) { d[2] = cvtpk(acc[p][NT - 1][0], acc[p][NT - 1][1]); d[3] = cvtpk(acc[p][NT - 1][2], acc[p][NT - 1][3]); }
          if (i == 18) __builtin_amdgcn_raw_buffer_store_b128(d, rsS, sbase + r * 2048 + 1024, 0, 16);
        } else {
          if (i == 14) {
            const u32x2 e = {cvtpk(acc[p][2][0], acc[p][2][1]), cvtpk(acc[p][2][2], acc[p][2][3])};
            if (r & 1) d = u32x4{keep[0], keep[1], e[0], e[1]}; else keep = e;
          }
          if (i == 17 && (r & 1)) __builtin_amdgcn_raw_buffer_store_b128(d, rsS, sbase + (5 + (r >> 1)) * 1024, 0, 16);
          if (i == 17 && r == 4) __builtin_amdgcn_raw_buffer_store_b64(keep, rsS, shalf, 0, 16);
        }
      }
      SBAR();
    }
    if (rt == 0) mid();
  }
}
DI void zero_acc(f32x4 (&acc)[4][5]) {
#pragma unroll
  for (int dt = 0; dt < 4; ++dt)
#pragma unroll
    for (int rt = 0; rt < 5; ++rt) acc[dt][rt] = f32x4{0.f, 0.f, 0.f, 0.f};
}
DI float read_D(int rt, unsigned fl, int row) {
  float d[4];
#pragma unroll
  for (int w = 0; w < 4; ++w) {
    const float v = ldsf(L_DLT + (w * 80 + rt * 16 + row) * 4);
    d[w] = ((fl >> (8 * w)) & 1) ? v : 0.f;
  }
  return fmaxf(fmaxf(d[0], d[1]), fmaxf(d[2], d[3]));
}
DI float fadd_s(float a, float b) { float r = a + b; asm("" : "+v"(r)); return r; }
DI unsigned ls_addr(int w, int rt, int row) { return (rt < 4 ? L_K0 + rt * 64 : L_K1) + w * KSLOT + 8192 + row * 4; }

constexpr unsigned long long mkord(const int (&o)[16]) { unsigned long long r = 0; for (int i = 0; i < 16; ++i) r |= (unsigned long long)o[i] << (4 * i); return r; }
constexpr int ORD0[16] = {0, 4, 1, 2, 3, 8, 5, 6, 7, 12, 9, 10, 11, 13, 14, 15};
constexpr int ORD1[16] = {1, 0, 5, 2, 3, 4, 9, 6, 7, 8, 13, 10, 11, 12, 14, 15};
constexpr int ORD2[16] = {2, 0, 1, 6, 3, 4, 5, 10, 7, 8, 9, 14, 11, 12, 13, 15};
constexpr int ORD3[16] = {3, 0, 1, 2, 7, 4, 5, 6, 11, 8, 9, 10, 15, 12, 13, 14};
#define INL __attribute__((always_inline))

__global__ __launch_bounds__(512, 2) void gqa_fused(const void* __restrict__ x, const void* __restrict__ kc, const void* __restrict__ vc,
                                                    const void* __restrict__ wq, const void* __restrict__ wo, char* ws, char* __restrict__ out) {
  STAMP(0);
  TA(0);
  const int wid = __builtin_amdgcn_readfirstlane(threadIdx.x >> 6);
  if (wid >= 4) {
    unsigned long long pc;
    asm volatile("s_getpc_b64 %0" : "=s"(pc));
    const rsrc_t rsC = __builtin_amdgcn_make_buffer_rsrc((void*)(pc & ~127ull), 0, CODE_BYTES, 0x00020000);
    asm volatile("s_mov_b32 m0, 0x8000\n\ts_nop 4\n\tbuffer_load_dword %0, %1, %2 offen lds" ::"v"((threadIdx.x & 63) * 128), "s"(rsC), "s"((wid - 4) * 8192) : "memory");
  }
  SBAR();
  const int bid = blockIdx.x, kvh = bid & 7, q = (bid >> 3) & 3, b = bid >> 5, hg = q >> 1, ph = q & 1, h = 2 * kvh + hg;
  const int w = wid & 3, hf = wid >> 2;
  const unsigned ks0 = L_K0 + w * KSLOT, ks1 = L_K1 + w * KSLOT, vra = L_VR + w * 16384, vrb = vra + 8192;
#define TILE(t) ((unsigned)(t))
  const rsrc_t rsZ = __builtin_amdgcn_make_buffer_rsrc((char*)wo, 0, 0, 0x00020000);
  const rsrc_t rsK = __builtin_amdgcn_make_buffer_rsrc((char*)kc + (size_t)(b * HKV + kvh) * SK * KVROW - 128, 0, ABL_KSZ(SK * KVROW + 128), 0x00020000);
  const rsrc_t rsV = __builtin_amdgcn_make_buffer_rsrc((char*)vc + (size_t)(b * HKV + kvh) * SK * KVROW, 0, ABL_VSZ(SK * KVROW), 0x00020000);
  unsigned dummy = 0;
#define SP() spacer(dummy, rsZ)
#define DMAK(P, slot, so)                                                                        \
  do {                                                                                           \
    if (((P) & 3) == 0) dma0<0>((slot) + ((P) >> 2) * 4224, kv0, rsK, (so) + ((P) >> 2) * 4096); \
    else dmaN<((P) & 3) * 1056>(kv0, rsK, (so) + ((P) >> 2) * 4096 - ((P) & 3) * 32);            \
  } while (0)

  bf16x8 kf[8];
  unsigned kv0, kra;
  {
    const rsrc_t rsX = __builtin_amdgcn_make_buffer_rsrc((char*)x + (size_t)(b * SQ + ph * 80) * XROW, 0, 80 * XROW, 0x00020000);
    const rsrc_t rsWQ = __builtin_amdgcn_make_buffer_rsrc((char*)wq, 0, DM * WQROW, 0x00020000);
    const int lane = lane_now();
    unsigned wv[4];
    unit_voff<true>(wv, lane, WQROW);
    const unsigned xv = (lane >> 3) * XROW + ((unsigned)((lane & 7) ^ (lane >> 4) ^ ((wid & 1) << 2)) << 4);
    const unsigned rot = (unsigned)(2 * b + ph) % 7u;
    auto chunk = [&](int s) INL { const unsigned c = rot + s; return c >= 7 ? c - 7 : c; };
    constexpr unsigned XS[7] = {L_K0, L_K0 + 10240, L_K0 + 20480, L_P0, L_P1, L_MS, L_P0};
    auto aslot = [&](int s) INL { return s % 3 == 0 ? vrb : s % 3 == 1 ? vra : ks1; };
    const unsigned wso = hf * 32 * WQROW + h * 512 + w * 128;
    auto req_x = [&](int s) INL {
      dma0<0>(XS[s] + wid * 1024, xv, rsX, wid * 8 * XROW + chunk(s) * 128);
      if (wid < 2) dma0<0>(XS[s] + (wid + 8) * 1024, xv, rsX, (wid + 8) * 8 * XROW + chunk(s) * 128);
    };
    TA(10);
    if (TOUCH == 2 && wid == 3) {
      const rsrc_t rsW = __builtin_amdgcn_make_buffer_rsrc(ws, 0, WS_SLAB + 256 * SLAB, 0x00020000);
      const int l = lane_now(), grp = b * 2 + ph;
      const unsigned o = l < 8 ? WS_FLAG + (grp * 8 + l) * 128 : l < 16 ? WS_EPOCH + (grp * 8 + l - 8) * 128 : l < 32 ? WS_SLAB + (grp * 16 + l - 16) * SLAB : 0x80000000u;
      asm volatile("buffer_load_dword %0, %1, %2, 0 offen sc1" : "+{v255}"(dummy) : "v"(o), "s"(rsW) : "memory");
    }
    req_x(0); dma_unit(aslot(0) + hf * 4096, wv, rsWQ, chunk(0) * 64 * WQROW + wso);
    asm volatile("s_barrier" ::: "memory");
    req_x(1); dma_unit(aslot(1) + hf * 4096, wv, rsWQ, chunk(1) * 64 * WQROW + wso);
    TA(1);
    kv0 = (lane >> 5) * 512 + (((lane & 31) ^ (lane >> 5)) << 4);
    kra = ks0 + ((lane & 15) >> 1) * 1056 + (lane & 1) * 512 + (((lane >> 4) ^ (lane & 1)) << 4);
    unsigned tv[2], tk[2], xk[2], xp[2];
#pragma unroll
    for (int i = 0; i < 2; ++i) {
      const unsigned o = off_tr<true>(lane, 2 * hf + i);
      tv[i] = vra + o; tk[i] = ks1 + o; xk[i] = off_x(lane) ^ (64 * i); xp[i] = L_P0 + xk[i];
      asm volatile("" : "+v"(tv[i]), "+v"(tk[i]), "+v"(xk[i]), "+v"(xp[i]));
    }
    f32x4 acc[2][5];
    bf16x8 a0[2], b0[5], a1[2], b1[5];
    auto stepA = [&](int mma, int RS, int RH, int DS, int DH, bf16x8 (&ca)[2], bf16x8 (&cb)[5], bf16x8 (&na)[2], bf16x8 (&nb)[5]) INL {
#pragma unroll
      for (int i = 0; i < 10; ++i) {
        if (mma == 1) mfma_acc(acc[i & 1][i >> 1], ca[i & 1], cb[i >> 1]);
        if (mma == 2) mfma_init(acc[i & 1][i >> 1], ca[i & 1], cb[i >> 1]);
        if (RS >= 0) {
          const unsigned xa = RS < 3 ? xk[RH] + XS[RS] : xp[RH] + (XS[RS] - L_P0);
          if (i == 0 || i == 2) na[i >> 1] = ldfrag<true>((RS % 3 == 2 ? tk[i >> 1] : tv[i >> 1] + (RS % 3 == 0 ? 8192 : 0)) + RH * 4096);
          if (i == 1) nb[0] = lds128(xa);
          if (i >= 3 && i <= 6) nb[i - 2] = lds128(xa + (i - 2) * 2048);
        }
        if (DS >= 0) {
          const unsigned so = chunk(DS) * 64 * WQROW + wso;
          if (i == 7) { if (DH == 0) dma0<0>(aslot(DS) + hf * 4096, wv[0], rsWQ, so); else dmaN<2048>(wv[2], rsWQ, so); }
          if (i == 9) { if (DH == 0) dmaN<1024>(wv[1], rsWQ, so); else dmaN<3072>(wv[3], rsWQ, so); }
        }
        SBAR();
      }
    };
    if (wid < 2) WAITVM(6); else WAITVM(5);
    BARRIER();
    TA(2);
    STAMP(1);
    TB(0);
    stepA(false, 0, 0, -1, 0, a1, b1, a0, b0);
    {
#pragma unroll
      for (int i = 0; i < 5; ++i) {
        const unsigned p = wid + 8 * i, cs = p / 10, j = p - cs * 10;
        const unsigned sl = cs == 0 ? XS[2] : L_P0 + (cs - 1) * 10240;
        unsigned c = rot + 2 + cs; c = c >= 7 ? c - 7 : c;
        dma0<0>(sl + j * 1024, xv, rsX, j * 8 * XROW + c * 128);
      }
      const unsigned so = chunk(2) * 64 * WQROW + wso;
      dma0<0>(aslot(2) + hf * 4096, wv[0], rsWQ, so); dmaN<1024>(wv[1], rsWQ, so);
      SBAR();
    }
    TB(1);
    stepA(2, 0, 1, 2, 1, a0, b0, a1, b1);
    TB(2);
    WAITVM(9);
    BARRIER();
    TB(3);
#pragma unroll
    for (int s = 1; s < 5; ++s) {
      if (s == 4) { req_x(6); SBAR(); }
      stepA(true, s, 0, s + 2, 0, a1, b1, a0, b0);
      if (s == 3) TB(10);
      stepA(true, s, 1, s + 2, 1, a0, b0, a1, b1);
      if (s == 1) TB(4);
      if (s == 2) TB(7);
      if (s == 3) TB(11);
      WAITVM(4);
      if (s == 1) TB(5);
      if (s == 2) TB(8);
      if (s == 3) TB(12);
      BARRIER();
      if (s == 1) TB(6);
      if (s == 2) TB(9);
      if (s == 3) TB(13);
      if (s == 4) TB(14);
    }
    TA(3);
    if (hf == 0) {
      const unsigned so = TILE(0) * 32768 + w * 8192 + 128;
      DMAK(0, ks0, so); DMAK(1, ks0, so); DMAK(2, ks0, so); DMAK(3, ks0, so);
      DMAK(4, ks0, so); DMAK(5, ks0, so); DMAK(6, ks0, so); DMAK(7, ks0, so);
    } else {
      unit_voff<false>(wv, lane, KVROW);
      dma_unit(vra, wv, rsV, TILE(0) * 32768 + w * 128);
      dma_unit(vra + 4096, wv, rsV, TILE(0) * 32768 + 16384 + w * 128);
    }
    SBAR();
    stepA(true, 5, 0, -1, 0, a1, b1, a0, b0);
    stepA(true, 5, 1, -1, 0, a0, b0, a1, b1);
    WAITVM(8);
    BARRIER();
    stepA(true, 6, 0, -1, 0, a1, b1, a0, b0);
    stepA(true, 6, 1, -1, 0, a0, b0, a1, b1);
    if (hf == 0) {
      WAITVM(0);
#pragma unroll
      for (int ks = 0; ks < 8; ++ks) kf[ks] = lds128(kra + ks * 64);
      SBAR();
    }
    stepA(true, -1, 0, -1, 0, a1, b1, a0, b0);
    TA(4);
    MFMA_DRAIN();
    const int ksq = 2 * w + hf, g = lane >> 4;
    const unsigned qa = (ksq < 6 ? L_K1 + ksq * 5120 : L_P1 + (ksq - 6) * 5120) + ((g >> 1) * 16 + (lane & 15)) * 16 + (g & 1) * 8;
#pragma unroll
    for (int rt = 0; rt < 5; ++rt)
#pragma unroll
      for (int u = 0; u < 2; ++u) {
        const f32x4 v = acc[u][rt] * QSCALE;
        *(LAS u32x2*)(qa + rt * 1024 + u * 512) = u32x2{cvtpk(v[0], v[1]), cvtpk(v[2], v[3])};
        SBAR();
      }
    if (wid == 4 && lane < 32) *(LAS unsigned*)(L_FLAG + lane * 4) = 0u;
    WAITLGKM0();
    TA(11);
    if (hf == 0) {
      const unsigned so = TILE(1) * 32768 + w * 8192 + 128;
      DMAK(0, ks0, so); DMAK(1, ks0, so); SP(); DMAK(2, ks0, so); DMAK(3, ks0, so); SP();
      DMAK(4, ks0, so); DMAK(5, ks0, so); SP(); DMAK(6, ks0, so); DMAK(7, ks0, so); SP();
    }
    TA(5);
    asm volatile("s_barrier" ::: "memory");
    TBSAVE(true);
    TA(6);
    STAMP(2);
  }

  const unsigned rotC = (unsigned)(2 * b + ph) & 3u;
  const rsrc_t rsWO = __builtin_amdgcn_make_buffer_rsrc((char*)wo + (size_t)h * DH * WOROW, 0, wid == 7 ? 0 : DH * WOROW, 0x00020000);
  unsigned long long epoch;
  gu64* const epw = (gu64*)(ws + WS_EPOCH + ((b * 2 + ph) * 8 + wid) * 128);
  const rsrc_t rsS = __builtin_amdgcn_make_buffer_rsrc(ws + WS_SLAB + (size_t)((b * 2 + ph) * 16 + h) * SLAB, 0, SLAB, 0x00020000);
  auto o_bases = [&](unsigned (&ob)[4], int lane) INL {
#pragma unroll
    for (int i = 0; i < 4; ++i) { const unsigned cc = (rotC + i) & 3; ob[i] = (cc == 3 ? L_OI3 : L_P0 + cc * 10240) + lane * 16; }
  };

  if (wid < 4) {
    unsigned pwa;
    bf16x8 qf[5][8];
    {
      const int lane = lane_now();
      pwa = L_P0 + (w >> 1) * 5120 + (PLAY == 0 ? (w & 1) * 512 + lane * 8 : lane * 16 + (w & 1) * 8);
      unsigned q0 = L_K1 + lane * 16, q1 = L_P1 + lane * 16;
      asm volatile("" : "+v"(q0), "+v"(q1));
#pragma unroll
      for (int rt = 0; rt < 5; ++rt)
#pragma unroll
        for (int ks = 0; ks < 8; ++ks) qf[rt][ks] = lds128((ks < 6 ? q0 + ks * 5120 : q1 + (ks - 6) * 5120) + rt * 1024);
      SBAR();
    }
    float lsum[5] = {0.f, 0.f, 0.f, 0.f, 0.f}, tsr[5] = {0.f, 0.f, 0.f, 0.f, 0.f};
    f32x4 S0 = {0.f, 0.f, 0.f, 0.f};
#pragma unroll
    for (int ks = 0; ks < 8; ++ks) S0 = mfma(kf[ks], qf[0][ks], S0);
    TA(7);
    unsigned dslot = ks1;
    const unsigned pwsum = 2 * pwa + 10240, krsum = 2 * kra + L_K1;
    auto qk_tile = [&](int t, auto wo_c) INL -> unsigned {
      constexpr bool WO = decltype(wo_c)::value;
      unsigned so, wv[4];
      if constexpr (WO) { unit_voff<false>(wv, lane_now(), WOROW); so = ((rotC + t - 14) & 3) * 64 * WOROW + w * 128; }
      else so = TILE(t + 2) * 32768 + w * 8192 + 128;
      unsigned fa = L_FLAG + t * 4;
      asm volatile("" : "+v"(fa));
      asm volatile("" : "+s"(so));
      TR(11);
      BARRIER();
      TR(12);
      TRSAVE(t == TRT + 1);
      if (t == 0) TA(8);
      if (t == 1) { TA(9); TASAVE(true); }
      unsigned fl;
      f32x4 S1;
      float p0, p1, p2, p3, tm;
      u32x2 pk;
#define QDMA(P)                                                                                                   \
  do {                                                                                                            \
    if constexpr (WO) {                                                                                           \
      if (((P) & 3) == 0) dma0<0>(dslot + ((P) >> 2) * 4096, wv[0], rsWO, so + ((P) >> 2) * 32 * WOROW);           \
      else dmaN<((P) & 3) * 1024>(wv[(P) & 3], rsWO, so + ((P) >> 2) * 32 * WOROW);                                \
    } else DMAK(P, dslot, so);                                                                                    \
  } while (0)
#define QM(i, RN, SN) if ((i) == 0) mfma_init(SN, kf[i], qf[RN][i]); else mfma_acc(SN, kf[i], qf[RN][i]);
#define QKBURST(RC, SC, RN, SN)                                                                                    \
  TR(2 * RC); QM(0, RN, SN) SBAR(); PRIO_HI(); p0 = ex2(SC[0]); SBAR();                                            \
  QM(1, RN, SN) SBAR(); p1 = ex2(SC[1]); SBAR();                                                                   \
  QM(2, RN, SN) SBAR(); p2 = ex2(SC[2]); SBAR();                                                                   \
  QM(3, RN, SN) SBAR(); p3 = ex2(SC[3]); SBAR();                                                                   \
  QM(4, RN, SN) SBAR(); pk[0] = cvtpk(p0, p1); p0 = fadd_s(p0, p1); SBAR();                                        \
  QM(5, RN, SN) SBAR(); pk[1] = cvtpk(p2, p3); p2 = fadd_s(p2, p3); SBAR();                                        \
  QM(6, RN, SN) SBAR(); lsum[RC] = fadd_s(lsum[RC], tsr[RC]); tsr[RC] = fadd_s(p0, p2); SBAR();                    \
  QM(7, RN, SN) SBAR(); PRIO_LO(); TR(2 * RC + 1);
#define QKFIN(RC)                                                                                                  \
  *(LAS u32x2*)(pwa + RC * 1024) = pk;                                                                             \
  tm = RC == 0 ? tsr[0] : fadd_s(tm, tsr[RC]); SBAR();
      QKBURST(0, S0, 1, S1)
      fl = *(LAS unsigned*)fa;
      QKFIN(0)
      QDMA(0); QDMA(1); SP(); SBAR();
      QKBURST(1, S1, 2, S0)
      QKFIN(1)
      unsigned long long bad = __ballot(fl != 0);
      QDMA(2); QDMA(3); SP(); SBAR();
      QKBURST(2, S0, 3, S1)
      QKFIN(2)
      QDMA(4); QDMA(5); SP(); SBAR();
      QKBURST(3, S1, 4, S0)
      WAITVM(10);
#pragma unroll
      for (int ks = 0; ks < 8; ++ks) kf[ks] = lds128(kra + ks * 64);
      kra = krsum - kra;
      asm volatile("" : "+v"(kra));
      SBAR();
      QKFIN(3)
      QKBURST(4, S0, 0, S1)
      QKFIN(4)
      bad |= __ballot(!(tm <= PTHR));
      pwa = pwsum - pwa;
      asm volatile("" : "+v"(pwa));
      SBAR();
      QDMA(6); QDMA(7); SP(); SBAR();
      dslot = ks0 + ks1 - dslot;
      S0 = S1;
#undef QKFIN
#undef QKBURST
#undef QM
#undef QDMA
      if (!WO && __builtin_expect(t == 0, 0)) bad |= __ballot(!(fminf(fminf(fminf(tsr[0], tsr[1]), fminf(tsr[2], tsr[3])), tsr[4]) >= PLOW));
      TR(10);
      return (unsigned)bad | (unsigned)(bad >> 32);
    };
    int t = -1;
    unsigned abn, last = 0x4000u;
#pragma unroll 1
    do { ++t; last >>= 1; asm volatile("" : "+s"(last)); abn = qk_tile(t, std::false_type{}); } while ((abn | (last & 1u)) == 0);
    if (abn == 0) {
#pragma unroll 1
      do { ++t; abn = qk_tile(t, std::true_type{}); } while ((abn | (t & 1)) == 0);
    }
    int entry = 2;
    if (abn == 0) {
#pragma unroll
      for (int rt = 0; rt < 5; ++rt) lsum[rt] += tsr[rt];
      t = 16;
      BARRIER();
      entry = __builtin_amdgcn_readfirstlane(*(LAS unsigned*)(L_FLAG + 64)) != 0;
    } else {
      kra = krsum - kra;
      pwa = pwsum - pwa;
    }
    if (__builtin_expect(entry != 0, 0)) {
      MFMA_DRAIN();
      float negm[5] = {0.f, 0.f, 0.f, 0.f, 0.f};
#pragma unroll 1
      for (;; ++t) {
        if (entry == 0) BARRIER();
        {
          const unsigned fl = __builtin_amdgcn_readfirstlane(*(LAS unsigned*)(L_FLAG + t * 4));
          if (fl != 0) {
            const unsigned pprev = pwsum - pwa;
            const int row = lane_now() & 15;
#pragma unroll
            for (int rt = 0; rt < 5; ++rt) {
              const float D = read_D(rt, fl, row);
              const float own = ((fl >> (8 * w)) & 1) ? ldsf(L_DLT + (w * 80 + rt * 16 + row) * 4) : 0.f;
              const float f = ex2(own - D);
              const u32x2 pk = *(LAS u32x2*)(pprev + rt * 1024);
              const float a0 = __uint_as_float(pk[0] << 16) * f, a1 = __uint_as_float(pk[0] & 0xffff0000u) * f;
              const float a2 = __uint_as_float(pk[1] << 16) * f, a3 = __uint_as_float(pk[1] & 0xffff0000u) * f;
              *(LAS u32x2*)(pprev + rt * 1024) = u32x2{cvtpk(a0, a1), cvtpk(a2, a3)};
              lsum[rt] *= f;
              negm[rt] -= D;
              SBAR();
            }
            BARRIER();
          }
        }
        if (t == 16) break;
        if (entry != 2) {
          const unsigned dslot = (t & 1) ? ks0 : ks1;
          if (t < 14) {
            const unsigned so = TILE(t + 2) * 32768 + w * 8192 + 128;
            DMAK(0, dslot, so); DMAK(1, dslot, so); DMAK(2, dslot, so); DMAK(3, dslot, so);
            DMAK(4, dslot, so); DMAK(5, dslot, so); DMAK(6, dslot, so); DMAK(7, dslot, so);
          } else {
            unsigned wv[4];
            unit_voff<false>(wv, lane_now(), WOROW);
            const unsigned so = ((rotC + t - 14) & 3) * 64 * WOROW + w * 128;
            dma_unit(dslot, wv, rsWO, so);
            dma_unit(dslot + 4096, wv, rsWO, so + 32 * WOROW);
          }
        } else {
          const int lane = lane_now();
#pragma unroll
          for (int ks = 0; ks < 8; ++ks)
            kf[ks] = __builtin_bit_cast(bf16x8, __builtin_amdgcn_raw_buffer_load_b128(rsK, (lane & 15) * 512 + (lane >> 4) * 16 + ks * 64, TILE(t) * 32768 + w * 8192 + 128, 0));
        }
        f32x4 S[5];
        float mx[5];
        bool need = false;
#pragma unroll
        for (int rt = 0; rt < 5; ++rt) {
          S[rt] = f32x4{negm[rt], negm[rt], negm[rt], negm[rt]};
#pragma unroll
          for (int ks = 0; ks < 8; ++ks) S[rt] = mfma(kf[ks], qf[rt][ks], S[rt]);
          mx[rt] = ceilf(xmax4(fmaxf(fmaxf(S[rt][0], S[rt][1]), fmaxf(S[rt][2], S[rt][3]))));
          need |= !(mx[rt] <= GTHR) || (t == 0 && !(mx[rt] >= -GTHR));
          SBAR();
        }
        need = __any(need);
        const int lane = lane_now();
#pragma unroll
        for (int rt = 0; rt < 5; ++rt) {
          const float dl = need ? (t > 0 ? fmaxf(mx[rt], 0.f) : mx[rt]) : 0.f;
          const float p0 = ex2(S[rt][0] - dl), p1 = ex2(S[rt][1] - dl), p2 = ex2(S[rt][2] - dl), p3 = ex2(S[rt][3] - dl);
          const float ts = (p0 + p1) + (p2 + p3);
          lsum[rt] = t > 0 ? lsum[rt] * ex2(-dl) + ts : ts;
          *(LAS u32x2*)(pwa + rt * 1024) = u32x2{cvtpk(p0, p1), cvtpk(p2, p3)};
          if (need && lane < 16) *(LAS float*)(L_DLT + (w * 80 + rt * 16 + lane) * 4) = dl;
          SBAR();
        }
        if (need) *(LAS unsigned char*)(L_FLAG + (t + 1) * 4 + w) = 1;
        WAITVM(8);
#pragma unroll
        for (int ks = 0; ks < 8; ++ks) kf[ks] = lds128(kra + ks * 64);
        kra = krsum - kra;
        pwa = pwsum - pwa;
        entry = 0;
      }
    }
    {
      const int lane = lane_now();
#pragma unroll
      for (int rt = 0; rt < 5; ++rt) {
        const float l = xsum4(lsum[rt]);
        if (lane < 16) *(LAS float*)ls_addr(w, rt, lane) = l;
      }
    }
    const int lane = lane_now();
    unsigned t1[4], wv[4], ob[4];
#pragma unroll
    for (int dt = 0; dt < 4; ++dt) t1[dt] = ks0 + off_tr<false>(lane, dt);
    unit_voff<false>(wv, lane, WOROW);
    o_bases(ob, lane);
    bf16x8 af[8][4];
    const unsigned so2 = ((rotC + 2) & 3) * 64 * WOROW, so3 = ((rotC + 3) & 3) * 64 * WOROW;
    WAITVM(8);
#pragma unroll
    for (int j = 0; j < 2; ++j)
#pragma unroll
      for (int dt = 0; dt < 4; ++dt) af[j][dt] = ldfrag<false>(t1[dt] + L_K1 + j * 4096);
    WAITLGKM0();
    dma_unit(ks1, wv, rsWO, so2 + w * 128); SP();
    dma_unit(ks1 + 4096, wv, rsWO, so2 + 32 * WOROW + w * 128); SP();
    WAITVM(10);
#pragma unroll
    for (int j = 2; j < 4; ++j)
#pragma unroll
      for (int dt = 0; dt < 4; ++dt) af[j][dt] = ldfrag<false>(t1[dt] + (j & 1) * 4096);
    BARRIER();
    STAMP(5);
    dma_unit(ks0, wv, rsWO, so3 + w * 128); SP();
    dma_unit(ks0 + 4096, wv, rsWO, so3 + 32 * WOROW + w * 128); SP();
#pragma unroll
    for (int j = 4; j < 8; ++j) {
      if (w < 3) WAITVM(16); else if (j == 4) WAITVM(16); else if (j == 5) WAITVM(11); else if (j == 6) WAITVM(6); else WAITVM(1);
#pragma unroll
      for (int dt = 0; dt < 4; ++dt) af[j][dt] = ldfrag<false>(t1[dt] + (j < 6 ? L_K1 : 0) + (j & 1) * 4096);
      WAITLGKM0();
      if (w < 3) { dma_unit((j < 6 ? ks1 : ks0) + (j & 1) * 4096, wv, rsWO, (j < 6 ? so2 : so3) + (j & 1) * 32 * WOROW + (4 + w) * 128); SP(); }
    }
    asm volatile("s_barrier" ::: "memory");
    STAMP(6);
    oproj<4>(af, ob, rsS, w * 10240 + lane * 16, 0, [&]() INL {
      WAITVM(0);
      asm volatile("s_barrier" ::: "memory");
      epoch = __hip_atomic_load(epw + (lane >> 6), __ATOMIC_RELAXED, __HIP_MEMORY_SCOPE_AGENT);
    });
    STAMP(7);
  } else {
    const int lane = lane_now(), row = lane & 15;
    unsigned ta[4], dv[4];
#pragma unroll
    for (int dt = 0; dt < 4; ++dt) ta[dt] = vra + off_tr<false>(lane, dt);
    unit_voff<false>(dv, lane, KVROW);
    if (TOUCH == 1 && wid == 7) {
      const rsrc_t rsW = __builtin_amdgcn_make_buffer_rsrc(ws, 0, WS_SLAB + 256 * SLAB, 0x00020000);
      const int l = lane_now(), grp = b * 2 + ph;
      const unsigned o = l < 8 ? WS_FLAG + (grp * 8 + l) * 128 : l < 16 ? WS_EPOCH + (grp * 8 + l - 8) * 128 : l < 32 ? WS_SLAB + (grp * 16 + l - 16) * SLAB : 0x80000000u;
      asm volatile("buffer_load_dword %0, %1, %2, 0 offen sc1" : "+{v255}"(dummy) : "v"(o), "s"(rsW) : "memory");
    }
    dma_unit(vrb, dv, rsV, TILE(1) * 32768 + w * 128);
    SP();
    dma_unit(vrb + 4096, dv, rsV, TILE(1) * 32768 + 16384 + w * 128);
    SP();
    f32x4 acc[4][5];
    zero_acc(acc);
    bf16x8 va[2][4], vb[2][4];
    WAITVM(10);
#pragma unroll
    for (int hh = 0; hh < 2; ++hh)
#pragma unroll
      for (int dt = 0; dt < 4; ++dt) va[hh][dt] = ldfrag<false>(ta[dt] + hh * 4096);
    unsigned pa = L_P0 + lane * (PLAY == 0 ? 8 : 16);
    asm volatile("" : "+v"(pa));
    TA(7);
    BARRIER();
    TA(8);
    TASAVE(true);
    rsrc_t rsD = rsV;
    unsigned dstep = 16384;
    auto pv_tile = [&](int t, auto par_c, bf16x8 (&vc)[2][4], bf16x8 (&vn)[2][4]) INL {
      constexpr int PAR = decltype(par_c)::value;
      unsigned so;
      if (t < 14) so = TILE(t + 2) * 32768 + w * 128;
      else so = ((rotC + t - 14) & 3) * 64 * WOROW + wid * 128;
      const unsigned dsl = vra + PAR * 8192;
      unsigned fa = L_FLAG + (t + 1) * 4;
      asm volatile("" : "+v"(fa));
      asm volatile("" : "+s"(so));
      bf16x8 pf[5][2];
#define RDP(rt) { pf[rt][0] = ldp(pa + PAR * 10240 + (rt) * 1024); pf[rt][1] = ldp(pa + PAR * 10240 + 5120 + (rt) * 1024); }
#define PM(i, rt) mfma_acc(acc[(i) & 3][rt], vc[(i) >> 2][(i) & 3], pf[rt][(i) >> 2]);
#define RDV(dt, hh) vn[hh][dt] = ldfrag<false>(ta[dt] + (PAR ^ 1) * 8192 + (hh) * 4096);
#define BURST(rt)                                                                                                  \
  SBAR(); TR(2 * rt + 1); PM(0, rt) SBAR(); PRIO_HI(); SBAR(); PM(1, rt) PM(2, rt) PM(3, rt) PM(4, rt) PM(5, rt) PM(6, rt) PM(7, rt) SBAR(); PRIO_LO(); TR(2 * rt + 2); SBAR();
#define PD(P)                                                                                                      \
  {                                                                                                                \
    if (((P) & 3) == 0) dma0<0>(dsl + ((P) >> 2) * 4096, dv[0], rsD, (P) ? so + dstep : so);                       \
    else dmaN<((P) & 3) * 1024>(dv[(P) & 3], rsD, (P) >> 2 ? so + dstep : so);                                     \
  }
      TR(11);
      BARRIER();
      TR(12);
      TRSAVE(t == TRT);
      TR(0);
      unsigned fl = *(LAS unsigned*)fa;
      RDP(0) RDP(1)
      SBAR();
      PD(0) SP();
      SBAR();
      fl = __builtin_amdgcn_readfirstlane(fl);
      if (__builtin_expect(fl != 0, 0)) {
        if (t > 0) {
#pragma unroll
          for (int rt = 0; rt < 5; ++rt) {
            const float f = ex2(-read_D(rt, fl, row));
#pragma unroll
            for (int dt = 0; dt < 4; ++dt) acc[dt][rt] *= f;
          }
        }
        BARRIER();
        RDP(0) RDP(1)
      }
      BURST(0)
      RDP(2) SBAR();
      PD(1) PD(2) SP();
      BURST(1)
      RDP(3) SBAR();
      WAITVM(11);
      RDV(0, 0) RDV(1, 0) SBAR();
      PD(3)
      BURST(2)
      RDP(4) RDV(2, 0) RDV(3, 0) SBAR();
      PD(4) PD(5) SP();
      if (PAR == 1 && t == 15) BARRIER();
      BURST(3)
      WAITVM(10);
      RDV(0, 1) RDV(1, 1) RDV(2, 1) RDV(3, 1) SBAR();
      PD(6) PD(7) SP();
      BURST(4)
#undef PD
#undef BURST
#undef RDV
#undef PM
#undef RDP
    };
#pragma unroll 1
    for (int t = 0; t < 16; t += 2) {
      if (t == 14) { unit_voff<false>(dv, lane_now(), WOROW); rsD = rsWO; dstep = 32 * WOROW; }
      pv_tile(t, std::integral_constant<int, 0>{}, va, vb);
      pv_tile(t + 1, std::integral_constant<int, 1>{}, vb, va);
    }
    STAMP(5);
    float inv[5];
#pragma unroll
    for (int rt = 0; rt < 5; ++rt) {
      float l = 0.f;
#pragma unroll
      for (int ww = 0; ww < 4; ++ww) l += ldsf(ls_addr(ww, rt, row));
      inv[rt] = __builtin_amdgcn_rcpf(l);
    }
    if (w == 3) WAITVM(0);
    MFMA_DRAIN();
    SBAR();
    const unsigned oa = (w == 3 ? L_OI3 : L_P0 + w * 10240) + lane * 16;
#pragma unroll
    for (int rt = 0; rt < 5; ++rt)
#pragma unroll
      for (int ks = 0; ks < 2; ++ks) {
        const f32x4 u = acc[2 * ks][rt] * inv[rt], v = acc[2 * ks + 1][rt] * inv[rt];
        *(LAS u32x4*)(oa + ks * 5120 + rt * 1024) = u32x4{cvtpk(u[0], u[1]), cvtpk(u[2], u[3]), cvtpk(v[0], v[1]), cvtpk(v[2], v[3])};
        SBAR();
      }
    unsigned ob[4], tA[3], tB[3];
    o_bases(ob, lane);
#pragma unroll
    for (int i = 0; i < 3; ++i) {
      const int T = 3 * w + i, o = T >> 2;
      const unsigned tr = off_tr<false>(lane, T & 3);
      tA[i] = L_VR + o * 16384 + tr; tB[i] = L_K0 + o * KSLOT + tr;
    }
    WAITVM(0);
    BARRIER();
    STAMP(6);
    bf16x8 af[8][3];
#pragma unroll
    for (int j = 0; j < 4; ++j)
#pragma unroll
      for (int i = 0; i < 3; ++i) af[j][i] = ldfrag<false>(tA[i] + j * 4096);
    asm volatile("s_barrier" ::: "memory");
#pragma unroll
    for (int j = 4; j < 8; ++j)
#pragma unroll
      for (int i = 0; i < 3; ++i) af[j][i] = ldfrag<false>(tB[i] + (j < 6 ? L_K1 : 0) + (j & 1) * 4096);
    oproj<3>(af, ob, rsS, 40960 + w * 7680 + lane * 16, 40960 + w * 7680 + 7168 + lane * 8, [&]() INL { epoch = __hip_atomic_load(epw + (lane >> 6), __ATOMIC_RELAXED, __HIP_MEMORY_SCOPE_AGENT); });
    STAMP(7);
  }
  {
    unsigned long long tag = epoch + 0x9E3779B97F4A7C15ull;
    tag = (tag ^ (tag >> 30)) * 0xBF58476D1CE4E5B9ull;
    tag = (tag ^ (tag >> 27)) * 0x94D049BB133111EBull;
    tag ^= tag >> 31;
    const int lane = lane_now();
    gu64* fl = (gu64*)(ws + WS_FLAG + ((b * 2 + ph) * 8 + wid) * 128);
    const int nch = hf ? 30 : 40, r = h * nch + (lane < nch ? lane : 0), piece = r >> 6;
    int nt0, nt1, rt0, rt1, l0 = r & 63, l1 = r & 63;
    if (hf == 0) {
      nt0 = 4 * w + 2 * (piece & 1); nt1 = nt0 + 1; rt0 = rt1 = piece >> 1;
    } else {
      nt0 = 16 + 3 * w + (piece < 5 ? 0 : 2); nt1 = 16 + 3 * w + (piece < 5 ? 1 : 2);
      rt0 = piece < 5 ? piece : 2 * (piece - 5); rt1 = piece < 5 ? piece : piece < 7 ? 2 * (piece - 5) + 1 : 4;
      if (piece == 7) { l0 = 2 * l0; l1 = l0 + 1; }
    }
    const unsigned d0 = ((16 * rt0 + (l0 & 15)) * DM + 16 * nt0 + 4 * (l0 >> 4)) * 2, d1 = ((16 * rt1 + (l1 & 15)) * DM + 16 * nt1 + 4 * (l1 >> 4)) * 2;
    const unsigned src = lane < nch ? (hf ? 40960 + w * 7680 : w * 10240) + r * 16 : 0x80000000u;
    const rsrc_t rsG = __builtin_amdgcn_make_buffer_rsrc(ws + WS_SLAB + (size_t)(b * 2 + ph) * 16 * SLAB, 0, 16 * SLAB, 0x00020000);
    unsigned long long f = ~tag;
    if (hf == 0) f = __hip_atomic_load(fl + (lane & 15), __ATOMIC_RELAXED, __HIP_MEMORY_SCOPE_AGENT);
    WAITVM(0);
    asm volatile("" :: "{v255}"(dummy));
    STAMP(12);
    if (lane == 0) __hip_atomic_store(fl + h, tag, __ATOMIC_RELAXED, __HIP_MEMORY_SCOPE_AGENT);
    u32x4 v[16];
#pragma unroll
    for (int hh = 0; hh < 16; ++hh) v[hh] = u32x4{0u, 0u, 0u, 0u};
    unsigned pend = 0xffffu;
#pragma unroll 1
    for (unsigned spin = 0; spin < (1u << 22); ++spin) {
      const unsigned rdy = ((unsigned)__ballot(f == tag) | (1u << h)) & pend;
      __builtin_amdgcn_fence(__ATOMIC_ACQUIRE, "wavefront");
      asm volatile("" ::: "memory");
#pragma unroll
      for (int hh = 0; hh < 16; ++hh)
        if ((rdy >> hh) & 1) v[hh] = __builtin_amdgcn_raw_buffer_load_b128(rsG, src, hh * SLAB, 16);
      pend &= ~rdy;
      if (pend == 0) break;
      if (rdy == 0) __builtin_amdgcn_s_sleep(SLP);
      f = __hip_atomic_load(fl + (lane & 15), __ATOMIC_RELAXED, __HIP_MEMORY_SCOPE_AGENT);
    }
    STAMP(13);
    if (lane < nch) {
      if (h == 0 && lane == 0) __hip_atomic_store(epw, epoch + 1, __ATOMIC_RELAXED, __HIP_MEMORY_SCOPE_AGENT);
      float sm[8] = {0.f, 0.f, 0.f, 0.f, 0.f, 0.f, 0.f, 0.f};
#pragma unroll
      for (int hh = 0; hh < 16; ++hh)
#pragma unroll
        for (int i = 0; i < 4; ++i) { sm[2 * i] += __uint_as_float(v[hh][i] << 16); sm[2 * i + 1] += __uint_as_float(v[hh][i] & 0xffff0000u); }
      char* dst = out + (size_t)(b * 2 + ph) * 80 * DM * 2;
      *(u32x2*)(dst + d0) = u32x2{cvtpk(sm[0], sm[1]), cvtpk(sm[2], sm[3])};
      *(u32x2*)(dst + d1) = u32x2{cvtpk(sm[4], sm[5]), cvtpk(sm[6], sm[7])};
    }
  }
}

extern "C" void kernel_launch(void* const* d_in, const int* in_sizes, int n_in, void* d_out, int out_size, void* d_ws, size_t ws_size,
                              hipStream_t stream) {
  (void)in_sizes; (void)n_in; (void)out_size; (void)ws_size;
  (void)hipFuncSetAttribute((const void*)gqa_fused, hipFuncAttributeMaxDynamicSharedMemorySize, L_TOTAL);
  gqa_fused<<<256, 512, L_TOTAL, stream>>>(d_in[0], d_in[1], d_in[2], d_in[3], d_in[4], (char*)d_ws, (char*)d_out);
}
```
